```python
import torch

def _softmax(x, axis=-1):
    return torch.softmax(x, dim=axis)
def _gelu(x):
    return torch.nn.functional.gelu(x, approximate="tanh")
def _silu(x):
    return torch.nn.functional.silu(x)
def _sigmoid(x):
    return torch.sigmoid(x)
def _layernorm(x, axis=-1, eps=1e-5):
    m = x.mean(dim=axis, keepdim=True)
    v = x.var(dim=axis, keepdim=True, unbiased=False)
    return (x - m) / torch.sqrt(v + eps)
def _scatter_add(tgt, idx, src, axis=0):
    return tgt.clone().index_add_(axis, idx, src)
def _segment_sum(src, seg, n):
    out = torch.zeros((n,) + src.shape[1:], dtype=src.dtype)
    return out.index_add_(0, seg, src)
def _pad(x, pw):
    flat = tuple(v for pair in reversed(pw) for v in pair)
    return torch.nn.functional.pad(x, flat)

kernel_name = 'parallel_sum__linear_kernel_hedgehog__dense_activation_relu__prompt'

def setup_inputs():

    _w = (lambda a: (lambda t: t.to(torch.bfloat16) if t.is_floating_point() else t)(torch.as_tensor(a)))
    def _A_setup():
        B, S, D, H = 1, 2048, 512, 32
        Dh = D // H
        return {
            "x": _w((lambda *s: torch.randn(*s, dtype=torch.bfloat16) * 0.05)(B, S, D)),
            "wqkv": _w((lambda *s: torch.randn(*s, dtype=torch.bfloat16) * 0.4)(D, 3 * D)),
            "w_phi": _w((lambda *s: torch.randn(*s, dtype=torch.bfloat16) * 0.4)(H, Dh, Dh) * (Dh ** -0.5)),
            "wo": _w((lambda *s: torch.randn(*s, dtype=torch.bfloat16) * 0.1)(D, D)),
        }
    _A_in = _A_setup()

    def _B_setup():
        B, S, D, Dff = 1, 2048, 512, 1792
        return {
            "x": _w((lambda *s: torch.randn(*s, dtype=torch.bfloat16) * 0.05)(B, S, D)),
            "w1": _w((lambda *s: torch.randn(*s, dtype=torch.bfloat16) * 0.05)(D, Dff)),
            "w2": _w((lambda *s: torch.randn(*s, dtype=torch.bfloat16) * 0.05)(Dff, D)),
        }
    _B_in = _B_setup()

    return {
        "A_x": _A_in["x"],
        "A_wqkv": _A_in["wqkv"],
        "A_w_phi": _A_in["w_phi"],
        "A_wo": _A_in["wo"],
        "B_x": _B_in["x"],
        "B_w1": _B_in["w1"],
        "B_w2": _B_in["w2"],
    }

def reference(A_x, A_wqkv, A_w_phi, A_wo, B_x, B_w1, B_w2):
    def _A_ref(x, wqkv, w_phi, wo):
        B, S, D = x.shape
        H = 32; Dh = D // H
        qkv = (lambda x, *d: x.permute(*d))((x @ wqkv).reshape(B, S, 3, H, Dh), 2, 0, 3, 1, 4)
        q, k, v = qkv[0], qkv[1], qkv[2]
        pq = torch.einsum("bhsd,hdf->bhsf", q, w_phi)
        pk = torch.einsum("bhsd,hdf->bhsf", k, w_phi)
        phi_q = torch.concatenate([_softmax(pq, axis=-1), _softmax(-pq, axis=-1)], axis=-1)
        phi_k = torch.concatenate([_softmax(pk, axis=-1), _softmax(-pk, axis=-1)], axis=-1)
        kv = torch.einsum("bhsf,bhsd->bhfd", phi_k, v)
        z  = torch.einsum("bhsf->bhf", phi_k)
        num = torch.einsum("bhqf,bhfd->bhqd", phi_q, kv)
        den = torch.einsum("bhqf,bhf->bhq", phi_q, z)[..., None] + 1e-6
        o = num / den
        return (lambda x, *d: x.permute(*d))(o, 0, 2, 1, 3).reshape(B, S, D) @ wo

    def _B_ref(x, w1, w2):
        h = torch.einsum("bsd,df->bsf", x, w1)
        zero = torch.zeros_like(h)
        h = torch.maximum(h, zero)
        return torch.einsum("bsf,fd->bsd", h, w2)

    x = A_x
    xn = _layernorm(x, axis=-1)
    return x + _A_ref(xn, A_wqkv, A_w_phi, A_wo) + _B_ref(xn, B_w1, B_w2)
```

```cpp
#include <hip/hip_runtime.h>
#include <hip/hip_bf16.h>
typedef unsigned short u16;
__device__ __forceinline__ float b2f(u16 b){ return __uint_as_float(((unsigned)b)<<16); }
__device__ __forceinline__ u16 f2b(float f){ unsigned u=__float_as_uint(f); u += 0x7fffu + ((u>>16)&1u); return (u16)(u>>16);}
__device__ __forceinline__ float rb(float f){ return b2f(f2b(f)); }
constexpr int S=2048,D=512,H=32,F=1792;
#define MIMIC 0
__global__ void k_ln(const u16* x, u16* xn){
  int r=blockIdx.x, t=threadIdx.x;
  float v[8]; float s=0;
  for(int i=0;i<8;i++){ v[i]=b2f(x[r*D+t*8+i]); s+=v[i]; }
  for(int o=32;o;o>>=1) s+=__shfl_xor(s,o);
  float m=s/D; float q=0;
  for(int i=0;i<8;i++){ float d=v[i]-m; q+=d*d; }
  for(int o=32;o;o>>=1) q+=__shfl_xor(q,o);
  float var=q/D;
  float rs=rsqrtf(var+1e-5f);
  for(int i=0;i<8;i++) xn[r*D+t*8+i]=f2b((v[i]-m)*rs);
}
__global__ void k_gemm(const u16* A, const u16* B, u16* C, int M,int N,int K,int relu){
  __shared__ float As[32][33], Bs[32][33];
  int tx=threadIdx.x, ty=threadIdx.y; int row=blockIdx.y*32+ty, col=blockIdx.x*32+tx; float acc=0;
  for(int k0=0;k0<K;k0+=32){
    As[ty][tx]=b2f(A[row*K+k0+tx]); Bs[ty][tx]=b2f(B[(k0+ty)*N+col]); __syncthreads();
    for(int k=0;k<32;k++) acc+=As[ty][k]*Bs[k][tx]; __syncthreads();
  }
  if(relu) acc=fmaxf(acc,0.f);
  C[row*N+col]=f2b(acc);
}
__global__ void k_phi(const u16* qkv, const u16* wphi, u16* phi){
  int idx=blockIdx.x*blockDim.x+threadIdx.x;
  int s=idx>>6, w=(idx>>5)&1, h=idx&31;
  float q[16]; for(int d=0;d<16;d++) q[d]=b2f(qkv[s*1536+w*512+h*16+d]);
  float p[16]; float mx=-1e30f, mn=1e30f;
  for(int f=0;f<16;f++){ float a=0; for(int d=0;d<16;d++) a+=q[d]*b2f(wphi[h*256+d*16+f]); p[f]=rb(a); mx=fmaxf(mx,p[f]); mn=fminf(mn,p[f]); }
  float e1[16],e2[16],s1=0,s2=0;
  for(int f=0;f<16;f++){ e1[f]=__expf(p[f]-mx); s1+=e1[f]; e2[f]=__expf(mn-p[f]); s2+=e2[f]; }
  for(int f=0;f<16;f++){ phi[idx*32+f]=f2b(e1[f]/s1); phi[idx*32+16+f]=f2b(e2[f]/s2);}
}
__global__ void k_kv(const u16* qkv, const u16* phi, float* kv){
  int h=blockIdx.x, t=threadIdx.x; int f=t/17, d=t%17; float a=0;
  for(int s=0;s<S;s++){ float p=b2f(phi[((s*2+1)*32+h)*32+f]); float v= d<16? b2f(qkv[s*1536+1024+h*16+d]):1.f; a+=p*v; }
  kv[h*544+t]=rb(a);
}
__global__ void k_o(const u16* phi, const float* kv, u16* o){
  int idx=blockIdx.x*blockDim.x+threadIdx.x; int s=idx>>9, h=(idx>>4)&31, d=idx&15;
  float num=0, den=0; for(int f=0;f<32;f++){ float p=b2f(phi[((s*2)*32+h)*32+f]); num+=p*kv[h*544+f*17+d]; den+=p*kv[h*544+f*17+16]; }
  o[idx]=f2b(rb(num)/rb(rb(den)+1e-6f));
}
__global__ void k_fin(const u16* x,const u16* a,const u16* b,u16* out){ int i=blockIdx.x*blockDim.x+threadIdx.x; out[i]=f2b(rb(b2f(x[i])+b2f(a[i]))+b2f(b[i])); }
extern "C" void kernel_launch(void* const* d_in, const int* in_sizes, int n_in, void* d_out, int out_size, void* d_ws, size_t ws_size, hipStream_t stream){
  const u16* x=(const u16*)d_in[0]; const u16* wqkv=(const u16*)d_in[1]; const u16* wphi=(const u16*)d_in[2]; const u16* wo=(const u16*)d_in[3];
  const u16* w1=(const u16*)d_in[5]; const u16* w2=(const u16*)d_in[6];
  char* ws=(char*)d_ws;
  u16* xn=(u16*)ws; u16* qkv=(u16*)(ws+(4<<20)); u16* hh=(u16*)(ws+(16<<20)); u16* phi=(u16*)(ws+(32<<20)); float* kv=(float*)(ws+(48<<20)); u16* o=(u16*)(ws+(52<<20)); u16* oa=(u16*)(ws+(56<<20)); u16* ob=(u16*)(ws+(60<<20));
  k_ln<<<S,64,0,stream>>>(x,xn);
  k_gemm<<<dim3(1536/32,S/32),dim3(32,32),0,stream>>>(xn,wqkv,qkv,S,1536,D,0);
  k_gemm<<<dim3(F/32,S/32),dim3(32,32),0,stream>>>(xn,w1,hh,S,F,D,1);
  k_phi<<<S*64/256,256,0,stream>>>(qkv,wphi,phi);
  k_kv<<<32,544,0,stream>>>(qkv,phi,kv);
  k_o<<<S*512/256,256,0,stream>>>(phi,kv,o);
  k_gemm<<<dim3(D/32,S/32),dim3(32,32),0,stream>>>(o,wo,oa,S,D,D,0);
  k_gemm<<<dim3(D/32,S/32),dim3(32,32),0,stream>>>(hh,w2,ob,S,D,F,0);
  k_fin<<<S*D/256,256,0,stream>>>(x,oa,ob,(u16*)d_out);
}
```

```cpp
#include <hip/hip_runtime.h>
#include <stdint.h>

typedef unsigned v4u __attribute__((ext_vector_type(4)));
typedef unsigned v2u __attribute__((ext_vector_type(2)));
typedef float f4 __attribute__((ext_vector_type(4)));
typedef float f2 __attribute__((ext_vector_type(2)));
typedef short s8 __attribute__((ext_vector_type(8)));
typedef short s4 __attribute__((ext_vector_type(4)));
typedef __bf16 bf2 __attribute__((ext_vector_type(2)));
typedef unsigned long long u64;
#define GAS __attribute__((address_space(1)))
#define LAS __attribute__((address_space(3)))
#define DI __device__ __forceinline__
#define RLX_AGENT __ATOMIC_RELAXED, __HIP_MEMORY_SCOPE_AGENT

#define PROFILE 0
#define KROT 1

constexpr unsigned WS_EPOCH = 0;
constexpr unsigned WS_FKV = 4096;
constexpr unsigned WS_FH = 8192;
constexpr unsigned WS_FO = 12288;
constexpr unsigned WS_Z = 65536;
constexpr unsigned WS_KV = 131072;
constexpr unsigned WS_A2 = WS_KV + (1u << 20);
constexpr unsigned WS_END = WS_A2 + 32u * 72u * 4096u;
constexpr unsigned WS_TS = 16u << 20;

constexpr unsigned L_MISC = 147456;
constexpr unsigned L_CS = L_MISC;
constexpr unsigned L_ST = L_MISC + 1024;
constexpr unsigned L_WPHI = L_MISC + 2048;
constexpr unsigned L_ZRED = L_MISC + 4096;
constexpr unsigned L_KVB = L_MISC + 6144;
constexpr unsigned L_ZB = L_MISC + 10240;
constexpr unsigned L_RED = 65536;
constexpr unsigned LDS_BYTES = 163840;

typedef __amdgpu_buffer_rsrc_t rsrc_t;
DI rsrc_t mk_rsrc(const void* p, unsigned bytes) { return __builtin_amdgcn_make_buffer_rsrc((void*)p, (short)0, (int)bytes, 0x00020000); }

DI void dma(unsigned m0v, unsigned voff, rsrc_t rs, unsigned soff) {
  asm volatile("s_mov_b32 m0, %0\n\ts_nop 0\n\tbuffer_load_dwordx4 %1, %2, %3 offen lds" ::"s"(m0v), "v"(voff), "s"(rs), "s"(soff) : "memory");
}
DI void dma_sc1(unsigned m0v, unsigned voff, rsrc_t rs, unsigned soff) {
  asm volatile("s_mov_b32 m0, %0\n\ts_nop 0\n\tbuffer_load_dwordx4 %1, %2, %3 offen sc1 lds" ::"s"(m0v), "v"(voff), "s"(rs), "s"(soff) : "memory");
}
#define WAIT_VM(n) asm volatile("s_waitcnt vmcnt(%0)" ::"i"(n) : "memory")
#define WAIT_LGKM0() do { __builtin_amdgcn_s_waitcnt(0xC07F); asm volatile("" ::: "memory"); } while (0)
#define BARRIER() asm volatile("s_barrier" ::: "memory")
#define SCHED() __builtin_amdgcn_sched_barrier(0)

DI f4 mfma32(v4u a, v4u b, f4 c) { return __builtin_amdgcn_mfma_f32_16x16x32_bf16(__builtin_bit_cast(s8, a), __builtin_bit_cast(s8, b), c, 0, 0, 0); }
DI f4 mfma16(v2u a, v2u b, f4 c) { return __builtin_amdgcn_mfma_f32_16x16x16bf16_1k(__builtin_bit_cast(s4, a), __builtin_bit_cast(s4, b), c, 0, 0, 0); }
DI unsigned pk2(float a, float b) { bf2 r = {(__bf16)a, (__bf16)b}; return __builtin_bit_cast(unsigned, r); }
DI v2u pk4(f4 v) { return v2u{pk2(v.x, v.y), pk2(v.z, v.w)}; }
DI float rbf(float a) { return __uint_as_float(pk2(a, 0.f) << 16); }
DI v2u trrd(unsigned addr) { return __builtin_bit_cast(v2u, __builtin_amdgcn_ds_read_tr16_b64_v4i16((LAS s4*)(uintptr_t)addr)); }
DI v4u trfrag(unsigned addr, unsigned off2) { v2u a = trrd(addr), b = trrd(addr + off2); return v4u{a.x, a.y, b.x, b.y}; }
template <class T> DI T ldsr(unsigned addr) { return *(LAS T*)(uintptr_t)addr; }
template <class T> DI void ldsw(unsigned addr, T v) { *(LAS T*)(uintptr_t)addr = v; }
DI unsigned rfl(unsigned x) { return __builtin_amdgcn_readfirstlane(x); }

DI float xg_add(float x) {
  auto a = __builtin_amdgcn_permlane16_swap(__float_as_uint(x), __float_as_uint(x), false, false);
  x = __uint_as_float(a[0]) + __uint_as_float(a[1]);
  auto b = __builtin_amdgcn_permlane32_swap(__float_as_uint(x), __float_as_uint(x), false, false);
  return __uint_as_float(b[0]) + __uint_as_float(b[1]);
}
DI float xg_max(float x) {
  auto a = __builtin_amdgcn_permlane16_swap(__float_as_uint(x), __float_as_uint(x), false, false);
  x = fmaxf(__uint_as_float(a[0]), __uint_as_float(a[1]));
  auto b = __builtin_amdgcn_permlane32_swap(__float_as_uint(x), __float_as_uint(x), false, false);
  return fmaxf(__uint_as_float(b[0]), __uint_as_float(b[1]));
}
DI v4u dual_softmax(f4 p) {
  constexpr float L2E = 1.4426950408889634f;
  float mx = xg_max(fmaxf(fmaxf(p.x, p.y), fmaxf(p.z, p.w)));
  float mn = -xg_max(-fminf(fminf(p.x, p.y), fminf(p.z, p.w)));
  const float a = -mx * L2E, b = mn * L2E;
  f4 e, n;
#pragma unroll
  for (int r = 0; r < 4; ++r) {
    e[r] = __builtin_amdgcn_exp2f(fmaf(p[r], L2E, a));
    n[r] = __builtin_amdgcn_exp2f(fmaf(p[r], -L2E, b));
  }
  const float se = __builtin_amdgcn_rcpf(xg_add((e.x + e.y) + (e.z + e.w)));
  const float sn = __builtin_amdgcn_rcpf(xg_add((n.x + n.y) + (n.z + n.w)));
  return v4u{pk2(e.x * se, e.y * se), pk2(e.z * se, e.w * se), pk2(n.x * sn, n.y * sn), pk2(n.z * sn, n.w * sn)};
}

DI void poll_flags(const char* ws, unsigned off, int n, u64 tag, int lane) {
  GAS u64* p = (GAS u64*)(ws + off) + (lane < n ? lane : 0);
  for (unsigned spins = 0; spins < (1u << 18); ++spins) {
    const u64 v = __hip_atomic_load(p, RLX_AGENT);
    if (__all(v == tag)) break;
    __builtin_amdgcn_s_sleep(1);
  }
  __builtin_amdgcn_fence(__ATOMIC_ACQUIRE, "wavefront");
}
DI void set_flag(char* ws, unsigned off, u64 tag) { __hip_atomic_store((GAS u64*)(ws + off), tag, RLX_AGENT); }

#define TS(k)

struct Ctx {
  rsrc_t rx, rw, rw2, rwo, rws;
  unsigned lane, w, m, hf, g;
  unsigned ti, jj, r2, c2, rot;
  unsigned vb2;
};

DI void issue_b2(const Ctx& c, int n) {
  const unsigned kq = c.w >> 1, kk = 2 * n + (c.w & 1);
  const unsigned dst = ((n + 3) & 3) * 32768u + c.w * 4096u;
  if (n < 7) {
    const unsigned hi = kk >= 7 ? 1u : 0u, t = kk - 7 * hi;
    const unsigned ua = t < 3 ? 2 * t : (t < 6 ? 2 * t + 1 : 6u), ub = t < 6 ? ua + 1 : 13u;
    const unsigned rb = 224u * (2 * kq + hi);
    const unsigned sa = (rb + 16 * ua) * 1024u, sb = (rb + 16 * ub) * 1024u;
    dma(dst, c.vb2, c.rw2, sa);
    dma(dst + 1024, c.vb2, c.rw2, sa + 8192);
    dma(dst + 2048, c.vb2, c.rw2, sb);
    dma(dst + 3072, c.vb2, c.rw2, sb + 8192);
  } else {
    const unsigned sa = (128u * kq + 32u * (kk - 14)) * 1024u;
#pragma unroll
    for (int p = 0; p < 4; ++p) dma(dst + p * 1024, c.vb2, c.rwo, sa + p * 8192);
  }
}
DI void issue_a2(const Ctx& c, int n) {
  const unsigned dst = (n & 3) * 32768u + c.w * 4096u;
  const unsigned so = WS_A2 + (c.r2 * 72u + 18u * (c.w >> 1) + 2 * n + (c.w & 1)) * 4096u;
#pragma unroll
  for (int p = 0; p < 4; ++p) dma_sc1(dst + p * 1024, c.lane * 16, c.rws, so + p * 1024);
}

template <int TYPE>
DI void phase1_loop(const Ctx& c, f4 (&acc)[7][2], f4 (&cs)[3], float& s1, float& s2, u64* ts) {
  constexpr int NU = TYPE ? 7 : 6, NI = TYPE ? 4 : 3;
  constexpr unsigned SL = TYPE ? 49152u : 40960u, WSTR = TYPE ? 3584u : 3072u, IMGS = TYPE ? 128u : 1024u;
  const unsigned lane = c.lane, w = c.w, g = c.g;
  const unsigned vx = (c.ti * 128 + 16 * w + (lane >> 3)) * 1024u + (((lane & 7) ^ (lane >> 3)) << 4);
  const unsigned fw = ((lane >> 4) & 1) | ((w & 1) << 1);
  const unsigned colb = TYPE ? ((448u * c.jj) & ~127u) : c.jj * 128u;
  const unsigned vw = (8 * w + (lane >> 3)) * WSTR + colb + (((lane & 7) ^ (fw << 1)) << 4);
  auto issue = [&](unsigned ch, unsigned slot) {
    const unsigned kc = (ch + c.rot) & 7;
    dma(slot + 2 * w * 1024, vx, c.rx, kc * 128);
    dma(slot + 2 * w * 1024 + 1024, vx, c.rx, kc * 128 + 8192);
#pragma unroll
    for (int im = 0; im < NI; ++im) dma(slot + 16384 + im * 8192 + w * 1024, vw, c.rw, kc * 64 * WSTR + im * IMGS);
  };
  issue(0, 0);
  issue(1, SL);
  issue(2, 2 * SL);
  const unsigned r15 = lane & 15;
  unsigned ax[2][2];
#pragma unroll
  for (int mi = 0; mi < 2; ++mi)
#pragma unroll
    for (int ks = 0; ks < 2; ++ks)
      ax[mi][ks] = (2 * (2 * c.m + (mi ^ c.hf)) + (r15 >> 3)) * 1024 + (r15 & 7) * 128 + (((4 * ks + g) ^ (r15 & 7)) << 4);
  const unsigned q = r15 >> 2, f = (q >> 1) | ((g & 1) << 1);
  const unsigned rowp = 16384 + (8 * g + q) * 128 + (lane & 3) * 8;
  constexpr int NA = TYPE ? 7 : 2;
  unsigned aw[NA];
  if (TYPE) {
#pragma unroll
    for (int t = 0; t < 7; ++t) {
      unsigned ap = (t >> 1) + c.m;
      ap = ap >= 3 ? ap - 3 : ap;
      const unsigned ua = 7 * c.hf + (t < 6 ? 2 * ap + (t & 1) : 6u) + 2 * (c.jj & 1);
      aw[t] = rowp + (ua >> 2) * 8192 + (((ua & 3) ^ f) << 5);
    }
  } else {
#pragma unroll
    for (int e = 0; e < 2; ++e) aw[e] = rowp + (((2 * c.hf + (e ^ (c.m & 1))) ^ f) << 5);
  }
  const v4u ones = {0x3f803f80u, 0x3f803f80u, 0x3f803f80u, 0x3f803f80u};
  v4u xa[2][2], wb[2][NU];
  auto rd = [&](int ks) {
#pragma unroll
    for (int mi = 0; mi < 2; ++mi) xa[ks][mi] = ldsr<v4u>(ax[mi][ks]);
#pragma unroll
    for (int u = 0; u < NU; ++u) wb[ks][u] = TYPE ? trfrag(aw[u] + ks * 4096, 512) : trfrag(aw[u & 1] + (u >> 1) * 8192 + ks * 4096, 512);
  };
  float t1 = 0.f, t2 = 0.f;
  auto mm = [&](int ks) {
#pragma unroll
    for (int u = 0; u < NU; ++u)
#pragma unroll
      for (int mi = 0; mi < 2; ++mi)
        acc[u][mi] = (TYPE == 0 && u >= 4) ? mfma32(xa[ks][mi], wb[ks][u], acc[u][mi]) : mfma32(wb[ks][u], xa[ks][mi], acc[u][mi]);
    if (TYPE) {
      cs[0] = mfma32(wb[ks][0], ones, cs[0]);
      cs[1] = mfma32(wb[ks][1], ones, cs[1]);
      cs[2] = mfma32(wb[ks][6], ones, cs[2]);
    } else {
      cs[0] = mfma32(wb[ks][0], ones, cs[0]);
      cs[1] = mfma32(wb[ks][2], ones, cs[1]);
      cs[2] = mfma32(ones, wb[ks][4], cs[2]);
    }
#pragma unroll
    for (int d = 0; d < 4; ++d) {
      const float lo = __uint_as_float(xa[ks][0][d] << 16), hi = __uint_as_float(xa[ks][0][d] & 0xffff0000u);
      s1 += lo;
      t1 += hi;
      s2 = fmaf(lo, lo, s2);
      t2 = fmaf(hi, hi, t2);
    }
  };
  TS(11);
  WAIT_VM(2 * (NI + 2));
  BARRIER();
  TS(12);
  rd(0);
  unsigned slot = 0;
#pragma unroll 1
  for (int ch = 0; ch < 8; ++ch) {
    WAIT_LGKM0();
    SCHED();
    rd(1);
    SCHED();
    mm(0);
    SCHED();
    WAIT_LGKM0();
    WAIT_VM(4);
    BARRIER();
    SCHED();
    if (ch < 5) {
      issue(ch + 3, slot);
    } else if (ch == 5) {
      issue_b2(c, 0);
    } else if (ch == 6) {
      issue_b2(c, 1);
    } else {
      issue_b2(c, 2);
    }
    const unsigned d = slot == 2 * SL ? (unsigned)(-2 * (int)SL) : SL;
    slot += d;
#pragma unroll
    for (int mi = 0; mi < 2; ++mi) { ax[mi][0] += d; ax[mi][1] += d; }
#pragma unroll
    for (int t = 0; t < NA; ++t) aw[t] += d;
    rd(0);
    SCHED();
    mm(1);
    SCHED();
  }
  s1 += t1;
  s2 += t2;
}

DI void publish_stats(const Ctx& c, float s1, float s2) {
  s1 = xg_add(s1);
  s2 = xg_add(s2);
  const float mean = s1 * (1.f / 512.f);
  const float var = fmaxf(s2 * (1.f / 512.f) - mean * mean, 0.f);
  const float mb = rbf(mean);
  const float sd = rbf(__builtin_amdgcn_sqrtf(rbf(rbf(var) + 1e-5f)));
  if (c.g == 0) ldsw<f2>(L_ST + (16 * (2 * c.m + c.hf) + c.lane) * 8, f2{mb, __builtin_amdgcn_rcpf(sd)});
}

__global__ __launch_bounds__(512, 2) void fused_block(const uint16_t* __restrict__ x, const uint16_t* __restrict__ wqkv,
                                                      const uint16_t* __restrict__ wphi, const uint16_t* __restrict__ wo,
                                                      const uint16_t* __restrict__ w1, const uint16_t* __restrict__ w2,
                                                      uint16_t* __restrict__ out, char* __restrict__ ws) {
  __shared__ __attribute__((aligned(1024))) char smem[LDS_BYTES];
  asm volatile("" ::"s"((unsigned)(uintptr_t)(LAS char*)smem));
  u64 ts[16];
#pragma unroll
  for (int i = 0; i < 16; ++i) ts[i] = 0;
  TS(0);
  Ctx c;
  const unsigned b = blockIdx.x, xcd = b & 7, idx = b >> 3;
  c.lane = threadIdx.x & 63;
  c.w = rfl(threadIdx.x >> 6);
  c.m = c.w & 3;
  c.hf = c.w >> 2;
  c.g = c.lane >> 4;
  c.ti = (xcd & 1) * 8 + (idx >> 2);
  const unsigned type = (idx >> 1) & 1;
  c.jj = (xcd >> 1) * 2 + (idx & 1);
  c.r2 = xcd * 4 + (idx >> 3);
  c.c2 = idx & 7;
  c.rot = KROT ? ((idx >> 2) + 2 * (idx & 3)) & 7 : 0;
  c.rx = mk_rsrc(x, 2048u * 1024u);
  c.rw = type ? mk_rsrc(w1, 512u * 3584u) : mk_rsrc(wqkv, 512u * 3072u);
  c.rw2 = mk_rsrc(w2, 1792u * 1024u);
  c.rwo = mk_rsrc(wo, 512u * 1024u);
  c.rws = mk_rsrc(ws, WS_END);
  c.vb2 = (c.lane >> 3) * 1024u + c.c2 * 128u + (((c.lane & 7) ^ (((c.lane >> 4) & 3) << 1)) << 4);
  const unsigned lane = c.lane, w = c.w, g = c.g, r15 = lane & 15;
  const u64 tag = *(const __attribute__((address_space(4))) u64*)(uintptr_t)(ws + WS_EPOCH) + 1;

  v4u phq[2][2];
  {
    f4 acc[7][2], cs[3];
#pragma unroll
    for (int u = 0; u < 7; ++u) acc[u][0] = acc[u][1] = f4{0.f, 0.f, 0.f, 0.f};
    cs[0] = cs[1] = cs[2] = f4{0.f, 0.f, 0.f, 0.f};
    float s1 = 0.f, s2 = 0.f;
    if (type) {
      phase1_loop<1>(c, acc, cs, s1, s2, ts);
      TS(1);
      publish_stats(c, s1, s2);
      unsigned ap[3];
#pragma unroll
      for (int p = 0; p < 3; ++p) { ap[p] = p + c.m; ap[p] = ap[p] >= 3 ? ap[p] - 3 : ap[p]; }
      if (r15 == 0) {
        ldsw<f4>(L_CS + (7 * c.hf + 2 * ap[0]) * 64 + g * 16, cs[0]);
        ldsw<f4>(L_CS + (7 * c.hf + 2 * ap[0] + 1) * 64 + g * 16, cs[1]);
      }
      WAIT_LGKM0();
      BARRIER();
      f2 st[2];
#pragma unroll
      for (int mi = 0; mi < 2; ++mi) st[mi] = ldsr<f2>(L_ST + (16 * (2 * c.m + (mi ^ c.hf)) + r15) * 8);
      f4 cu[7];
      cu[0] = cs[0]; cu[1] = cs[1]; cu[6] = cs[2];
#pragma unroll
      for (int t = 2; t < 6; ++t) cu[t] = ldsr<f4>(L_CS + (7 * c.hf + 2 * ap[t >> 1] + (t & 1)) * 64 + g * 16);
      const unsigned kb = 18 * (c.jj >> 1) + 7 * (c.jj & 1);
#pragma unroll
      for (int mi = 0; mi < 2; ++mi) {
        const unsigned mf = 2 * c.m + (mi ^ c.hf);
        const unsigned ob = WS_A2 + ((2 * c.ti + (mf >> 2)) * 72 + kb) * 4096 + (mf & 3) * 1024;
        v2u hp[7];
#pragma unroll
        for (int t = 0; t < 7; ++t) {
          f4 v;
#pragma unroll
          for (int r = 0; r < 4; ++r) v[r] = fmaxf((acc[t][mi][r] - st[mi].x * cu[t][r]) * st[mi].y, 0.f);
          hp[t] = pk4(v);
        }
#pragma unroll
        for (int p = 0; p < 3; ++p)
          __builtin_amdgcn_raw_buffer_store_b128(v4u{hp[2 * p].x, hp[2 * p].y, hp[2 * p + 1].x, hp[2 * p + 1].y}, c.rws,
                                                 lane * 16 + ob + (3 * c.hf + ap[p]) * 4096, 0, 16);
        __builtin_amdgcn_raw_buffer_store_b64(hp[6], c.rws, lane * 16 + c.hf * 8 + ob + 6 * 4096, 0, 16);
      }
      TS(2);
      WAIT_VM(8);
      phq[0][0] = phq[0][1] = phq[1][0] = phq[1][1] = v4u{0, 0, 0, 0};
    } else {
      if (w < 2) dma(L_WPHI + w * 1024, lane * 16, mk_rsrc(wphi, 32u * 512u), c.jj * 2048 + w * 1024);
      phase1_loop<0>(c, acc, cs, s1, s2, ts);
      TS(1);
      publish_stats(c, s1, s2);
      const unsigned h0 = 2 * c.hf + (c.m & 1), h1 = h0 ^ 1;
      if (r15 == 0) {
        ldsw<f4>(L_CS + h0 * 64 + g * 16, cs[0]);
        ldsw<f4>(L_CS + (4 + h0) * 64 + g * 16, cs[1]);
      }
      if (g == 0) ldsw<float>(L_CS + (8 + h0) * 64 + lane * 4, cs[2].x);
      WAIT_LGKM0();
      BARRIER();
      f2 st[2];
      f4 sv[2][2];
#pragma unroll
      for (int mi = 0; mi < 2; ++mi) {
        const unsigned rb = L_ST + 16 * (2 * c.m + (mi ^ c.hf)) * 8;
        st[mi] = ldsr<f2>(rb + r15 * 8);
        sv[mi][0] = ldsr<f4>(rb + g * 32);
        sv[mi][1] = ldsr<f4>(rb + g * 32 + 16);
      }
      f4 cq[2], ck[2];
      float cv[2];
      cq[0] = cs[0]; ck[0] = cs[1]; cv[0] = cs[2].x;
      cq[1] = ldsr<f4>(L_CS + h1 * 64 + g * 16);
      ck[1] = ldsr<f4>(L_CS + (4 + h1) * 64 + g * 16);
      cv[1] = ldsr<float>(L_CS + (8 + h1) * 64 + r15 * 4);
      v2u wp[2];
      wp[0] = trrd(L_WPHI + h0 * 512 + (4 * g + (r15 >> 2)) * 32 + (lane & 3) * 8);
      wp[1] = trrd(L_WPHI + h1 * 512 + (4 * g + (r15 >> 2)) * 32 + (lane & 3) * 8);
      const unsigned dg = r15 - 4 * g;
      const v2u ident = {dg == 0 ? 0x3f80u : (dg == 1 ? 0x3f800000u : 0u), dg == 2 ? 0x3f80u : (dg == 3 ? 0x3f800000u : 0u)};
      const v4u ones = {0x3f803f80u, 0x3f803f80u, 0x3f803f80u, 0x3f803f80u};
      const f4 zero = {0.f, 0.f, 0.f, 0.f};
#pragma unroll
      for (int e = 0; e < 2; ++e) {
        v2u tp[2], tn[2], vb[2];
#pragma unroll
        for (int mi = 0; mi < 2; ++mi) {
          f4 kx, vx;
#pragma unroll
          for (int r = 0; r < 4; ++r) {
            kx[r] = (acc[2 + e][mi][r] - st[mi].x * ck[e][r]) * st[mi].y;
            vx[r] = (acc[4 + e][mi][r] - sv[mi][r >> 1][(r & 1) * 2] * cv[e]) * sv[mi][r >> 1][(r & 1) * 2 + 1];
          }
          const v4u ph = dual_softmax(mfma16(wp[e], pk4(kx), zero));
          tp[mi] = pk4(mfma16(v2u{ph.x, ph.y}, ident, zero));
          tn[mi] = pk4(mfma16(v2u{ph.z, ph.w}, ident, zero));
          vb[mi] = pk4(vx);
        }
        const v4u ap = {tp[0].x, tp[0].y, tp[1].x, tp[1].y}, an = {tn[0].x, tn[0].y, tn[1].x, tn[1].y};
        const v4u bv = {vb[0].x, vb[0].y, vb[1].x, vb[1].y};
        const unsigned tl = 2 * (e ? h1 : h0);
        ldsw<f4>(L_RED + (tl * 4 + c.m) * 1024 + lane * 16, mfma32(ap, bv, zero));
        ldsw<f4>(L_RED + ((tl + 1) * 4 + c.m) * 1024 + lane * 16, mfma32(an, bv, zero));
        const f4 zp = mfma32(ap, ones, zero), zn = mfma32(an, ones, zero);
        if (r15 == 0) {
          ldsw<f4>(L_ZRED + (tl * 4 + c.m) * 64 + g * 16, zp);
          ldsw<f4>(L_ZRED + ((tl + 1) * 4 + c.m) * 64 + g * 16, zn);
        }
      }
      WAIT_LGKM0();
      BARRIER();
      {
        f4 s = ldsr<f4>(L_RED + w * 4096 + lane * 16), z = ldsr<f4>(L_ZRED + w * 256 + (lane & 3) * 16);
#pragma unroll
        for (int q = 1; q < 4; ++q) {
          s += ldsr<f4>(L_RED + w * 4096 + q * 1024 + lane * 16);
          z += ldsr<f4>(L_ZRED + w * 256 + q * 64 + (lane & 3) * 16);
        }
        __builtin_amdgcn_raw_buffer_store_b128(__builtin_bit_cast(v4u, s), c.rws, lane * 16 + WS_KV + ((c.jj * 8 + w) * 16 + c.ti) * 1024, 0, 16);
        if (lane < 4) __builtin_amdgcn_raw_buffer_store_b128(__builtin_bit_cast(v4u, z), c.rws, lane * 16 + WS_Z + ((c.jj * 8 + w) * 16 + c.ti) * 64, 0, 16);
      }
      TS(2);
#pragma unroll
      for (int e = 0; e < 2; ++e)
#pragma unroll
        for (int mi = 0; mi < 2; ++mi) {
          f4 qx;
#pragma unroll
          for (int r = 0; r < 4; ++r) qx[r] = (acc[e][mi][r] - st[mi].x * cq[e][r]) * st[mi].y;
          phq[e][mi] = dual_softmax(mfma16(wp[e], pk4(qx), zero));
        }
      WAIT_VM(0);
    }
  }
  const unsigned nh = w & 1, kq = w >> 1;
  v4u b2[18][2];
  unsigned ab2[2];
  {
    const unsigned q = r15 >> 2, f = (q >> 1) | ((g & 1) << 1);
#pragma unroll
    for (int nf = 0; nf < 2; ++nf) ab2[nf] = kq * 8192 + (4 * g + q) * 128 + (((2 * nh + nf) ^ f) << 5) + (lane & 3) * 8;
  }
#define READ_B2(n)                                                                                          \
  _Pragma("unroll") for (int e = 0; e < 2; ++e) _Pragma("unroll") for (int nf = 0; nf < 2; ++nf)             \
      b2[2 * (n) + e][nf] = trfrag(ab2[nf] + (((n) + 3) & 3) * 32768 + e * 4096, 2048)
  BARRIER();
  if (!type && threadIdx.x == 0) set_flag(ws, WS_FKV + c.jj * 256 + c.ti * 8, tag);
  TS(3);
  issue_b2(c, 3);
  READ_B2(0);
  READ_B2(1);
  READ_B2(2);
  WAIT_LGKM0();
  BARRIER();
  issue_b2(c, 4);
  issue_b2(c, 5);
  issue_b2(c, 6);
  if (type) {
    WAIT_VM(16);
    BARRIER();
    if (threadIdx.x == 0) set_flag(ws, WS_FH + c.ti * 256 + c.jj * 8, tag);
    TS(4);
    WAIT_VM(0);
  } else {
    if (w == 0) poll_flags(ws, WS_FKV + c.jj * 256, 16, tag, lane);
    BARRIER();
    TS(4);
    {
      v4u pv[16];
#pragma unroll
      for (int i = 0; i < 16; ++i) pv[i] = __builtin_amdgcn_raw_buffer_load_b128(c.rws, lane * 16, WS_KV + ((c.jj * 8 + w) * 16 + i) * 1024, 16);
      v4u zv = __builtin_amdgcn_raw_buffer_load_b128(c.rws, lane * 16, WS_Z + (c.jj * 8 + w) * 1024, 16);
      f4 s = __builtin_bit_cast(f4, pv[0]);
#pragma unroll
      for (int i = 1; i < 16; ++i) s += __builtin_bit_cast(f4, pv[i]);
      f4 z = __builtin_bit_cast(f4, zv);
#pragma unroll
      for (int r = 0; r < 4; ++r) {
        float t = z[r];
        t += __shfl_xor(t, 4);
        t += __shfl_xor(t, 8);
        z[r] = xg_add(t);
      }
      ldsw<v2u>(L_KVB + (w >> 1) * 1024 + lane * 16 + (w & 1) * 8, pk4(s));
      if (lane < 4) ldsw<v2u>(L_ZB + (w >> 1) * 64 + lane * 16 + (w & 1) * 8, pk4(z));
    }
    WAIT_LGKM0();
    BARRIER();
    TS(5);
    {
      const unsigned h0 = 2 * c.hf + (c.m & 1), h1 = h0 ^ 1;
      const f4 zero = {0.f, 0.f, 0.f, 0.f};
      v4u kf[2], zf[2];
      kf[0] = ldsr<v4u>(L_KVB + h0 * 1024 + lane * 16);
      kf[1] = ldsr<v4u>(L_KVB + h1 * 1024 + lane * 16);
      zf[0] = ldsr<v4u>(L_ZB + h0 * 64 + g * 16);
      zf[1] = ldsr<v4u>(L_ZB + h1 * 64 + g * 16);
      const unsigned kb = 18 * (c.jj >> 1) + 14 + 2 * (c.jj & 1) + c.hf;
#pragma unroll
      for (int mi = 0; mi < 2; ++mi) {
        v2u o[2];
#pragma unroll
        for (int e = 0; e < 2; ++e) {
          const f4 num = mfma32(kf[e], phq[e][mi], zero), den = mfma32(zf[e], phq[e][mi], zero);
          const float rc = __builtin_amdgcn_rcpf(den.x + 1e-6f);
          o[e] = pk4(num * rc);
        }
        const bool sw = c.m & 1;
        const v4u ov = sw ? v4u{o[1].x, o[1].y, o[0].x, o[0].y} : v4u{o[0].x, o[0].y, o[1].x, o[1].y};
        const unsigned mf = 2 * c.m + (mi ^ c.hf);
        __builtin_amdgcn_raw_buffer_store_b128(ov, c.rws, lane * 16 + WS_A2 + ((2 * c.ti + (mf >> 2)) * 72 + kb) * 4096 + (mf & 3) * 1024, 0, 16);
      }
    }
    WAIT_VM(2);
  }
  BARRIER();
  READ_B2(3);
  READ_B2(4);
  READ_B2(5);
  READ_B2(6);
  WAIT_LGKM0();
  BARRIER();
  issue_b2(c, 7);
  issue_b2(c, 8);
  if (!type) {
    WAIT_VM(8);
    BARRIER();
    if (threadIdx.x == 0) set_flag(ws, WS_FO + c.ti * 256 + c.jj * 8, tag);
  }
  TS(6);
  if (w == 0) poll_flags(ws, WS_FH + (c.r2 >> 1) * 256, 8, tag, lane);
  BARRIER();
  TS(7);
  issue_a2(c, 0);
  issue_a2(c, 1);
  WAIT_VM(8);
  BARRIER();
  READ_B2(7);
  READ_B2(8);
  WAIT_LGKM0();
  BARRIER();
  issue_a2(c, 2);
  f4 acc2[4][2];
#pragma unroll
  for (int s = 0; s < 4; ++s) acc2[s][0] = acc2[s][1] = f4{0.f, 0.f, 0.f, 0.f};
  unsigned aa[4];
#pragma unroll
  for (int s = 0; s < 4; ++s) aa[s] = kq * 8192 + ((s ^ kq) << 10) + lane * 16;
  v2u xr[2];
#pragma unroll
  for (int n = 0; n < 9; ++n) {
    if (n == 4 && w == 0) poll_flags(ws, WS_FO + (c.r2 >> 1) * 256, 8, tag, lane);
    if (n < 7) WAIT_VM(8);
    else if (n == 7) WAIT_VM(6);
    else WAIT_VM(2);
    BARRIER();
    if (n == 4) TS(8);
    if (n + 3 < 9) issue_a2(c, n + 3);
    if (n == 6) {
      const unsigned xo = (c.r2 * 64 + kq * 16 + r15) * 1024 + (c.c2 * 64 + nh * 32 + 4 * g) * 2;
      asm volatile("buffer_load_dwordx2 %0, %2, %3, 0 offen\n\tbuffer_load_dwordx2 %1, %2, %3, 0 offen offset:32"
                   : "=&v"(xr[0]), "=&v"(xr[1])
                   : "v"(xo), "s"(c.rx)
                   : "memory");
    }
    v4u af[2][4];
#pragma unroll
    for (int e = 0; e < 2; ++e)
#pragma unroll
      for (int s = 0; s < 4; ++s) af[e][s] = ldsr<v4u>(aa[s] + (n & 3) * 32768 + e * 4096);
#pragma unroll
    for (int e = 0; e < 2; ++e)
#pragma unroll
      for (int s = 0; s < 4; ++s)
#pragma unroll
        for (int nf = 0; nf < 2; ++nf) acc2[s][nf] = mfma32(b2[2 * n + e][nf], af[e][s], acc2[s][nf]);
    WAIT_LGKM0();
  }
  TS(9);
  if (b == 0 && threadIdx.x == 0) set_flag(ws, WS_EPOCH, tag);
#pragma unroll
  for (int s = 1; s < 4; ++s)
#pragma unroll
    for (int nf = 0; nf < 2; ++nf) ldsw<f4>(32768 + ((((kq ^ s) * 2 + nh) * 3 + (s - 1)) * 2 + nf) * 1024 + lane * 16, acc2[s][nf]);
  WAIT_LGKM0();
  BARRIER();
  asm volatile("s_waitcnt vmcnt(0)" : "+v"(xr[0]), "+v"(xr[1]) :: "memory");
  v2u o[2];
#pragma unroll
  for (int nf = 0; nf < 2; ++nf) {
    f4 s = acc2[0][nf];
#pragma unroll
    for (int q = 0; q < 3; ++q) s += ldsr<f4>(32768 + ((w * 3 + q) * 2 + nf) * 1024 + lane * 16);
    s.x += __uint_as_float(xr[nf].x << 16);
    s.y += __uint_as_float(xr[nf].x & 0xffff0000u);
    s.z += __uint_as_float(xr[nf].y << 16);
    s.w += __uint_as_float(xr[nf].y & 0xffff0000u);
    o[nf] = pk4(s);
  }
  {
    auto sx = __builtin_amdgcn_permlane16_swap(o[0].x, o[1].x, false, false);
    auto sy = __builtin_amdgcn_permlane16_swap(o[0].y, o[1].y, false, false);
    const unsigned col = c.c2 * 64 + nh * 32 + (g & 1) * 16 + (g >> 1) * 8;
    *(v4u*)(out + (size_t)(c.r2 * 64 + kq * 16 + r15) * 512 + col) = v4u{sx[0], sy[0], sx[1], sy[1]};
  }
}

extern "C" void kernel_launch(void* const* d_in, const int* in_sizes, int n_in, void* d_out, int out_size, void* d_ws,
                              size_t ws_size, hipStream_t stream) {
  fused_block<<<256, 512, 0, stream>>>((const uint16_t*)d_in[0], (const uint16_t*)d_in[1], (const uint16_t*)d_in[2],
                                       (const uint16_t*)d_in[3], (const uint16_t*)d_in[5], (const uint16_t*)d_in[6],
                                       (uint16_t*)d_out, (char*)d_ws);
}
```

```cpp
#include <hip/hip_runtime.h>
#include <stdint.h>

typedef unsigned v4u __attribute__((ext_vector_type(4)));
typedef unsigned v2u __attribute__((ext_vector_type(2)));
typedef float f4 __attribute__((ext_vector_type(4)));
typedef float f2 __attribute__((ext_vector_type(2)));
typedef float f8v __attribute__((ext_vector_type(8)));
typedef float f16v __attribute__((ext_vector_type(16)));
typedef short s8 __attribute__((ext_vector_type(8)));
typedef short s4 __attribute__((ext_vector_type(4)));
typedef short s2 __attribute__((ext_vector_type(2)));
typedef __bf16 bf2 __attribute__((ext_vector_type(2)));
typedef unsigned long long u64;
#define GAS __attribute__((address_space(1)))
#define LAS __attribute__((address_space(3)))
#define DI __device__ __forceinline__
#define RLX_AGENT __ATOMIC_RELAXED, __HIP_MEMORY_SCOPE_AGENT

#define PROFILE 0
#define CODE_PF 0

constexpr unsigned WS_EPOCH = 0;
constexpr unsigned WS_FKV = 4096;
constexpr unsigned WS_FA = 8192;
constexpr unsigned WS_ST = 16384;
constexpr unsigned WS_Z = 65536;
constexpr unsigned WS_CS = 131072;
constexpr unsigned WS_KV = 393216;
constexpr unsigned WS_A2 = WS_KV + (1u << 19);
constexpr unsigned WS_END = WS_A2 + 32u * 72u * 4096u;
constexpr unsigned WS_TS = 16u << 20;

constexpr unsigned L_GST = 131072;
constexpr unsigned L_MISC = 147456;
constexpr unsigned L_CSB = L_MISC;
constexpr unsigned L_STM = L_MISC + 2048;
constexpr unsigned L_STR = L_MISC + 3072;
constexpr unsigned L_STD = L_MISC + 3584;
constexpr unsigned L_WPHI = L_MISC + 4096;
constexpr unsigned L_ZRED = L_MISC + 6144;
constexpr unsigned L_ZERO = L_MISC + 8192;
constexpr unsigned L_KVB = L_MISC + 10240;
constexpr unsigned L_ZB = L_MISC + 14336;
constexpr unsigned L_SDB = L_MISC + 14592;
constexpr unsigned LDS_BYTES = 163840;

typedef __amdgpu_buffer_rsrc_t rsrc_t;
DI rsrc_t mk_rsrc(const void* p, unsigned bytes) { return __builtin_amdgcn_make_buffer_rsrc((void*)p, (short)0, (int)bytes, 0x00020000); }

template <unsigned MC, unsigned SC, bool SC1 = false> DI void dma(unsigned mb, unsigned voff, rsrc_t rs, unsigned sb) {
  unsigned t;
  if constexpr (SC1)
    asm volatile("s_add_u32 m0, %1, %2\n\ts_add_u32 %0, %3, %4\n\tbuffer_load_dwordx4 %5, %6, %0 offen sc1 lds"
                 : "=&s"(t) : "s"(mb), "i"(MC), "s"(sb), "i"(SC), "v"(voff), "s"(rs) : "memory", "scc");
  else
    asm volatile("s_add_u32 m0, %1, %2\n\ts_add_u32 %0, %3, %4\n\tbuffer_load_dwordx4 %5, %6, %0 offen lds"
                 : "=&s"(t) : "s"(mb), "i"(MC), "s"(sb), "i"(SC), "v"(voff), "s"(rs) : "memory", "scc");
}
#define WAIT_VM(n) asm volatile("s_waitcnt vmcnt(%0)" ::"i"(n) : "memory")
#define WAIT_LGKM0() do { __builtin_amdgcn_s_waitcnt(0xC07F); asm volatile("" ::: "memory"); } while (0)
#define BARRIER() asm volatile("s_barrier" ::: "memory")
#define SCHED() __builtin_amdgcn_sched_barrier(0)
#define PIN(x) asm volatile("" : "+v"(x))

DI f4 mfma32(v4u a, v4u b, f4 c) { return __builtin_amdgcn_mfma_f32_16x16x32_bf16(__builtin_bit_cast(s8, a), __builtin_bit_cast(s8, b), c, 0, 0, 0); }
DI f4 mfma16(v2u a, v2u b, f4 c) { return __builtin_amdgcn_mfma_f32_16x16x16bf16_1k(__builtin_bit_cast(s4, a), __builtin_bit_cast(s4, b), c, 0, 0, 0); }
DI f16v mfmaB(v4u a, v4u b, f16v c) { return __builtin_amdgcn_mfma_f32_32x32x16_bf16(__builtin_bit_cast(s8, a), __builtin_bit_cast(s8, b), c, 0, 0, 0); }
DI f16v mfmaB8(v2u a, v2u b, f16v c) { return __builtin_amdgcn_mfma_f32_32x32x8bf16_1k(__builtin_bit_cast(s4, a), __builtin_bit_cast(s4, b), c, 0, 0, 0); }
DI unsigned pk2(float a, float b) { bf2 r = {(__bf16)a, (__bf16)b}; return __builtin_bit_cast(unsigned, r); }
DI v2u pk4(f4 v) { return v2u{pk2(v.x, v.y), pk2(v.z, v.w)}; }
DI v4u pk8(const f16v& v, int o) { return v4u{pk2(v[o], v[o + 1]), pk2(v[o + 2], v[o + 3]), pk2(v[o + 4], v[o + 5]), pk2(v[o + 6], v[o + 7])}; }
DI unsigned relu2(unsigned p) { return __builtin_bit_cast(unsigned, __builtin_elementwise_max(__builtin_bit_cast(s2, p), s2{0, 0})); }
DI float rbf(float a) { return __uint_as_float(pk2(a, 0.f) << 16); }
DI float blo(unsigned p) { return __uint_as_float(p << 16); }
DI float bhi(unsigned p) { return __uint_as_float(p & 0xffff0000u); }
DI v2u trrd(unsigned addr) { return __builtin_bit_cast(v2u, __builtin_amdgcn_ds_read_tr16_b64_v4i16((LAS s4*)(uintptr_t)addr)); }
DI v4u cat(v2u a, v2u b) { return v4u{a.x, a.y, b.x, b.y}; }
DI v4u trfrag(unsigned addr, unsigned off2) { return cat(trrd(addr), trrd(addr + off2)); }
template <class T> DI T ldsr(unsigned addr) { return *(LAS T*)(uintptr_t)addr; }
template <class T> DI void ldsw(unsigned addr, T v) { *(LAS T*)(uintptr_t)addr = v; }
DI unsigned rfl(unsigned x) { return __builtin_amdgcn_readfirstlane(x); }
DI float max3(float a, float b, float c) { return __builtin_elementwise_maximum(__builtin_elementwise_maximum(a, b), c); }
DI float min3(float a, float b, float c) { return __builtin_elementwise_minimum(__builtin_elementwise_minimum(a, b), c); }
template <int CTRL> DI float dpp_add(float x) { return x + __builtin_bit_cast(float, __builtin_amdgcn_update_dpp(0, __builtin_bit_cast(int, x), CTRL, 0xf, 0xf, false)); }

DI float xg_add(float x) {
  auto a = __builtin_amdgcn_permlane16_swap(__float_as_uint(x), __float_as_uint(x), false, false);
  x = __uint_as_float(a[0]) + __uint_as_float(a[1]);
  auto b = __builtin_amdgcn_permlane32_swap(__float_as_uint(x), __float_as_uint(x), false, false);
  return __uint_as_float(b[0]) + __uint_as_float(b[1]);
}
DI float wave_add(float x) { return xg_add(dpp_add<0x121>(dpp_add<0x122>(dpp_add<0x124>(dpp_add<0x128>(x))))); }
DI float sum16(const f16v& v) {
  const f8v a = __builtin_shufflevector(v, v, 0, 1, 2, 3, 4, 5, 6, 7) + __builtin_shufflevector(v, v, 8, 9, 10, 11, 12, 13, 14, 15);
  const f4 b = __builtin_shufflevector(a, a, 0, 1, 2, 3) + __builtin_shufflevector(a, a, 4, 5, 6, 7);
  const f2 c = __builtin_shufflevector(b, b, 0, 1) + __builtin_shufflevector(b, b, 2, 3);
  return c.x + c.y;
}
DI float max16(const f16v& p) {
  return max3(max3(max3(p[0], p[1], p[2]), max3(p[3], p[4], p[5]), max3(p[6], p[7], p[8])),
              max3(max3(p[9], p[10], p[11]), max3(p[12], p[13], p[14]), p[15]), p[15]);
}
DI float min16(const f16v& p) {
  return min3(min3(min3(p[0], p[1], p[2]), min3(p[3], p[4], p[5]), min3(p[6], p[7], p[8])),
              min3(min3(p[9], p[10], p[11]), min3(p[12], p[13], p[14]), p[15]), p[15]);
}
DI void dual_softmax(const f16v& p, float rs, float os, v4u (&o)[4]) {
  const float sc = rs * 1.4426950408889634f;
  const float a = -max16(p) * sc, b = min16(p) * sc;
  f16v e, n;
#pragma unroll
  for (int r = 0; r < 16; ++r) {
    e[r] = __builtin_amdgcn_exp2f(__builtin_fmaf(p[r], sc, a));
    n[r] = __builtin_amdgcn_exp2f(__builtin_fmaf(p[r], -sc, b));
  }
  e *= __builtin_amdgcn_rcpf(sum16(e)) * os;
  n *= __builtin_amdgcn_rcpf(sum16(n)) * os;
  o[0] = pk8(e, 0);
  o[1] = pk8(e, 8);
  o[2] = pk8(n, 0);
  o[3] = pk8(n, 8);
}

constexpr unsigned SPIN_CAP = 1u << 18;
DI void set_flag(char* ws, unsigned off, u64 tag) { __hip_atomic_store((GAS u64*)(ws + off), tag, RLX_AGENT); }

#define TS(k)

enum Kind { P1, P2, WW, AA, KV, RED, END };
struct Item { int kind, n; };
#define SEQ_B {WW,0},{WW,1},{WW,2},{WW,3},{WW,4},{AA,0},{WW,5},{AA,1},{WW,6},{AA,2},{WW,7},{AA,3},{WW,8},{AA,4},{AA,5},{AA,6},{AA,7},{AA,8}
#define SEQ_A {AA,0},{WW,4},{AA,1},{WW,5},{AA,2},{WW,6},{AA,3},{WW,7},{AA,4},{WW,8},{AA,5},{AA,6},{AA,7},{AA,8}
constexpr Item seqB[] = {{P1,0},{P1,1},{P1,2},{P1,3},{P1,4},{P1,5},{P1,6},{P1,7},{P2,0},{P2,1},{P2,2},{P2,3}, SEQ_B};
constexpr Item seqA[] = {{P1,0},{P1,1},{P1,2},{P1,3},{P1,4},{P1,5},{P1,6},{P1,7},{WW,0},{RED,0},{P2,0},{P2,1},{WW,1},{WW,2},{WW,3},{KV,0},{KV,1}, SEQ_A};
template <int T> constexpr int nitems() { return T ? (int)(sizeof(seqB) / sizeof(Item)) : (int)(sizeof(seqA) / sizeof(Item)); }
template <int T> constexpr Item item_at(int k) { return k < 0 || k >= nitems<T>() ? Item{END, 0} : (T ? seqB[k] : seqA[k]); }
template <int T> constexpr int nops(int k) { return item_at<T>(k).kind == END || item_at<T>(k).kind == RED ? 0 : 4; }
template <int T> constexpr int gate_of(int k) {
  const Item it = item_at<T>(k);
  return it.kind == AA ? (it.n == 0 ? 0 : (it.n == 4 ? 1 : (it.n == 7 ? 2 : -1))) : -1;
}
template <int T> constexpr int pend(int k) { return item_at<T>(k - 1).kind == AA ? item_at<T>(k - 1).n : -1; }
template <int T> constexpr int lenient(int K) {
  if (K == 0) return -3;
  if (K == 1) return 1;
  if (T) return (K == 9 || K == 10 || (K >= 13 && K <= 15)) ? 4 : 0;
  return (K == 10 || K == 18) ? 2 : 0;
}

struct St {
  rsrc_t rx, rw, rw2, rwo, rws;
  char* ws;
  u64 tag;
  unsigned lane, w, m, hf, g, r15, ti, jj, r2, c2;
  unsigned m1, m2, m4;
  unsigned sp1, sp2, sw1, sw2, swo, sa2, skv;
  unsigned vx0, vx1, vw, vb2, vl;
  unsigned ax[4], aw[4], aw2[3], ab2[2], aa[4];
  v4u X[8][4];
  v2u wb[2][12][2];
  f4 acc[4][2], acc1[3][2];
  f16v kk, vv, qq;
  v4u wp0, wp1, ph[4];
  f4 kvs;
  v4u b2[18][2], af[2][8];
  f4 acc2[4][2];
  v2u gate[3], xr[2], gst;
  u64* ts;
};

template <int T, int K, int p> DI void issue_piece(St& c) {
  constexpr Item it = item_at<T>(K);
  constexpr unsigned sl = (K & 3) * 32768u, WSTR = T ? 3584u : 3072u, n = it.n;
  if constexpr (it.kind == P1) {
    if constexpr (p < 2) dma<sl + p * 1024, n * 128>(c.m2, p ? c.vx1 : c.vx0, c.rx, 0);
    else dma<sl + 16384 + (p - 2) * 8192, n * 64 * WSTR + (p - 2) * (T ? 128 : 1024)>(c.m1, c.vw, c.rw, c.sp1);
  } else if constexpr (it.kind == P2) {
    dma<sl + p * 8192, (T ? 2 * n + (p >> 1) : 4 * n + p) * 64 * WSTR + (T ? (p & 1) * 128 : 0)>(c.m1, c.vw, c.rw, c.sp2);
  } else if constexpr (it.kind == WW) {
    if constexpr (n < 4) dma<sl + p * 1024, (32 * n + 8 * p) * 1024>(c.m4, c.vb2, c.rw2, c.sw1);
    else if constexpr (n < 7) dma<sl + p * 1024, (16 * (p < 2 ? (n == 4 ? 0 : (n == 5 ? 3 : 2)) : (n == 4 ? 1 : (n == 5 ? 4 : 5))) + 8 * (p & 1)) * 1024>(c.m4, c.vb2, c.rw2, c.sw2);
    else dma<sl + p * 1024, (32 * (n - 7) + 16 * (p & 1) + 8 * (p >> 1)) * 1024>(c.m4, c.vb2, c.rwo, c.swo);
  } else if constexpr (it.kind == AA) {
    dma<sl + p * 1024, n * 4096 + p * 1024, true>(c.m4, c.vl, c.rws, c.sa2);
  } else if constexpr (it.kind == KV) {
    dma<sl + p * 1024, n * 256 + p * 2048, true>(c.m4, c.g * 512 + ((c.r15 ^ (c.g << 1)) << 4), c.rws, c.skv);
  }
}
template <int T, int K> DI void issue_rt(St& c, int p) {
  if (p == 0) issue_piece<T, K, 0>(c);
  if (p == 1) issue_piece<T, K, 1>(c);
  if (p == 2) issue_piece<T, K, 2>(c);
  if (p == 3) issue_piece<T, K, 3>(c);
}
template <int T, int K> DI void issue(St& c) {
  issue_piece<T, K, 0>(c);
  issue_piece<T, K, 1>(c);
  issue_piece<T, K, 2>(c);
  issue_piece<T, K, 3>(c);
}
DI void snapshot(St& c, v2u& r, unsigned addr) { asm volatile("buffer_load_dwordx2 %0, %1, %2, 0 offen sc1" : "=v"(r) : "v"(addr), "s"(c.rws) : "memory"); }
template <int N> DI void gate(St& c, v2u& r, unsigned addr) {
  asm volatile("s_waitcnt vmcnt(%1)" : "+v"(r) : "i"(N) : "memory");
  if (!__all(r.x == (unsigned)c.tag && r.y == (unsigned)(c.tag >> 32))) {
    GAS u64* p = (GAS u64*)(c.ws + addr);
#pragma unroll 1
    for (unsigned spins = 0; spins < SPIN_CAP; ++spins) {
      if (__all(__hip_atomic_load(p, RLX_AGENT) == c.tag)) break;
      __builtin_amdgcn_s_sleep(2);
    }
  }
  asm volatile("" ::: "memory");
}
template <int T> DI void gload(St& c) {
  const unsigned col = T ? 1536 + 224 * c.jj + 32 * c.w : (c.w < 6 ? c.w >> 1 : 2u) * 512 + 64 * c.jj + 32 * (c.w & 1);
  dma<L_GST, WS_CS, true>(c.m2, c.vl, c.rws, col * 64);
  dma<L_GST + 1024, WS_CS + 1024, true>(c.m2, c.vl, c.rws, col * 64);
  if (c.w < 2) dma<L_ZRED, WS_ST, true>(c.m1, c.vl, c.rws, c.ti * 1024);
}
template <int T> DI void gjob(St& c) {
  const unsigned lane = c.lane, w = c.w, tg = (unsigned)c.tag;
  v4u g0, g1;
  v2u gs;
#pragma unroll 1
  for (unsigned spins = 0; spins < SPIN_CAP; ++spins) {
    g0 = ldsr<v4u>(L_GST + threadIdx.x * 32);
    g1 = ldsr<v4u>(L_GST + threadIdx.x * 32 + 16);
    gs = ldsr<v2u>(L_ZRED + (w & 1) * 1024 + (threadIdx.x & 127) * 8);
    const bool ok = ((g0.y == tg && g0.w == tg && g1.y == tg && g1.w == tg) || (T && w == 7)) && (gs.y == tg || w >= 2);
    if (__all(ok)) break;
    gload<T>(c);
    WAIT_VM(0);
  }
  const float s = dpp_add<0xB1>((__uint_as_float(g0.x) + __uint_as_float(g0.z)) + (__uint_as_float(g1.x) + __uint_as_float(g1.z)));
  const float hi = rbf(s), mid = rbf(s - hi);
  if (!(lane & 1)) ldsw<v2u>(L_CSB + (threadIdx.x >> 1) * 8, v2u{pk2(hi, mid), pk2((s - hi) - mid, 0.f)});
  if (w < 2) {
    const unsigned nm = (gs.x & 0xffffu) ^ 0x8000u;
    ldsw<v2u>(L_STM + threadIdx.x * 8, v2u{nm | (nm << 16), nm});
    if (!T) {
      ldsw<float>(L_STR + threadIdx.x * 4, __builtin_amdgcn_rcpf(bhi(gs.x)));
      ldsw<float>(L_STD + threadIdx.x * 4, bhi(gs.x));
      ldsw<uint16_t>(L_SDB + threadIdx.x * 2, (uint16_t)(gs.x >> 16));
    }
  }
  SCHED();
}

template <int T, int K> DI void window(St& c) {
  constexpr Item it = item_at<T>(K);
  constexpr unsigned sl = (K & 1) * 32768u;
  constexpr unsigned hi = (K & 2) * 32768u, sla = sl + hi;
  constexpr int base = nops<T>(K + 1) + nops<T>(K + 2);
  constexpr int K2 = T ? 12 : 8;
  const unsigned lane = c.lane, w = c.w, g = c.g, r15 = c.r15, r31 = lane & 31, h = lane >> 5;
  u64* ts = c.ts;
  WAIT_LGKM0();
  WAIT_VM(base + lenient<T>(K));
  BARRIER();
  SCHED();
  if constexpr (K == 0) TS(12);
  if constexpr (K == 8) TS(1);
  if constexpr (K == (T ? 12 : 9)) TS(2);
  if constexpr (it.kind == AA && it.n == 0) TS(7);
  if constexpr (it.kind == AA && it.n == 4) TS(8);
  if constexpr (it.kind == AA && it.n == 7) TS(13);
  if constexpr (it.kind == KV && it.n == 0) TS(5);
  if constexpr (T && K == 11) { if (threadIdx.x == 0) set_flag(c.ws, WS_FA + c.ti * 512 + c.jj * 8, c.tag); TS(3); }
  if constexpr (T && K == 16) { if (threadIdx.x == 0) set_flag(c.ws, WS_FA + c.ti * 512 + 128 + c.jj * 8, c.tag); TS(4); }
  if constexpr (!T && K == 11) { if (threadIdx.x == 0) set_flag(c.ws, WS_FKV + c.jj * 256 + c.ti * 8, c.tag); TS(3); }
  if constexpr (!T && K == 19) { if (threadIdx.x == 0) set_flag(c.ws, WS_FA + c.ti * 512 + 256 + c.jj * 8, c.tag); TS(6); }
  if constexpr (gate_of<T>(K + 3) >= 0) {
    constexpr int gt = gate_of<T>(K + 3);
    gate<base>(c, c.gate[gt], WS_FA + (c.r2 >> 1) * 512 + gt * 128 + w * 8);
    if constexpr (gt == 0 && T) TS(5);
    if constexpr (gt == 1 && T) TS(6);
    if constexpr (gt == 2) TS(14);
  }
  if constexpr (gate_of<T>(K + 5) >= 0) snapshot(c, c.gate[gate_of<T>(K + 5)], WS_FA + (c.r2 >> 1) * 512 + gate_of<T>(K + 5) * 128 + w * 8);
  if constexpr (K == 5) gload<T>(c);
  if constexpr (K == nitems<T>() - 4) {
    const unsigned row = c.r2 * 64 + (w >> 1) * 16 + r15;
    const unsigned xo = row * 1024 + (c.c2 * 64 + (w & 1) * 32 + 4 * g) * 2;
    c.xr[0] = __builtin_amdgcn_raw_buffer_load_b64(c.rx, xo, 0, 0);
    c.xr[1] = __builtin_amdgcn_raw_buffer_load_b64(c.rx, xo + 32, 0, 0);
    c.gst = __builtin_amdgcn_raw_buffer_load_b64(c.rws, WS_ST + row * 8, 0, 16);
    asm volatile("" ::: "memory");
  }
  if constexpr (K > 0 && !(K & 1)) {
    if constexpr (K < 8) {
#pragma unroll
      for (int t = 0; t < (T ? 2 : 4); ++t) { c.ax[t] ^= 65536u; PIN(c.ax[t]); }
    }
    if constexpr (K < (T ? 8 : 12)) {
#pragma unroll
      for (int t = 0; t < (T ? 4 : 1); ++t) { c.aw[t] ^= 65536u; PIN(c.aw[t]); }
    }
    if constexpr (T && K == 10) {
#pragma unroll
      for (int t = 0; t < 3; ++t) { c.aw2[t] ^= 65536u; PIN(c.aw2[t]); }
    }
    if constexpr (K > K2) {
#pragma unroll
      for (int t = 0; t < 2; ++t) { c.ab2[t] ^= 65536u; PIN(c.ab2[t]); }
    }
    if constexpr (K > 16) {
#pragma unroll
      for (int t = 0; t < 4; ++t) { c.aa[t] ^= 65536u; PIN(c.aa[t]); }
    }
  }
  if constexpr (T && K == 8) {
    const unsigned q = r15 >> 2;
#pragma unroll
    for (int t = 0; t < 3; ++t) {
      const unsigned lu = (c.jj & 1) * 2 + 3 * c.hf + t;
      c.aw2[t] = (lu >> 2) * 8192 + (8 * g + q) * 128 + (lane & 3) * 8 + (((lu & 3) ^ ((q & 2) | (g & 1))) << 5);
    }
  }
  if constexpr (K == K2) {
    const unsigned q = r15 >> 2;
#pragma unroll
    for (int nf = 0; nf < 2; ++nf) c.ab2[nf] = (w >> 1) * 8192 + (4 * g + q) * 128 + (((2 * (w & 1) + nf) ^ ((q >> 1) | ((g & 1) << 1))) << 5) + (lane & 3) * 8;
  }
  if constexpr (K == 16) {
#pragma unroll
    for (int s = 0; s < 4; ++s) c.aa[s] = (w >> 1) * 8192 + ((s ^ (w >> 1)) << 10) + lane * 16;
  }
  static_assert(!(it.kind == AA && K < 16), "aa is formed in window 16");
  const f4 z4f = {0.f, 0.f, 0.f, 0.f};
  f16v zero;
#pragma unroll
  for (int r = 0; r < 16; ++r) zero[r] = 0.f;

  if constexpr (K == 7) { if (c.hf) gjob<T>(c); }
  if constexpr (T && K <= 12) {
    constexpr int cb = K & 1, pb = cb ^ 1;
    auto rd = [&](int i) {
      if constexpr (K < 8) {
        if (i < 4) c.X[K & 7][i] = ldsr<v4u>(c.ax[i >> 1] + sl + (i & 1) * 2048);
        else if (i < 20) c.wb[cb][(i - 4) >> 1][i & 1] = trrd(c.aw[((i - 4) >> 1) & 3] + sl + ((i - 4) >> 3) * 4096 + (i & 1) * 512);
      } else if constexpr (K < 12) {
        if (i < 24) c.wb[cb][i >> 1][i & 1] = trrd(c.aw2[(i >> 1) % 3] + sl + (i / 12) * 16384 + ((i / 6) & 1) * 4096 + (i & 1) * 512);
      }
    };
    constexpr int NM = K <= 8 ? 16 : 24;
    auto mm = [&](int i) {
      const v4u wf = cat(c.wb[pb][i >> 1][0], c.wb[pb][i >> 1][1]);
      if constexpr (K >= 1 && K <= 8) c.acc[(i >> 1) & 3][i & 1] = mfma32(wf, c.X[(K - 1) & 7][2 * (i >> 3) + (i & 1)], K == 1 && i < 8 ? z4f : c.acc[(i >> 1) & 3][i & 1]);
      if constexpr (K > 8) c.acc1[(i >> 1) % 3][i & 1] = mfma32(wf, c.X[(2 * (K - 9) + i / 12) & 7][2 * ((i / 6) & 1) + (i & 1)], K == 9 && i < 6 ? z4f : c.acc1[(i >> 1) % 3][i & 1]);
    };
    constexpr bool EP = K == 8 || K == 12;
    constexpr int NUN = K == 8 ? 4 : 3;
    v2u cf[4], nm[2];
    if constexpr (EP) {
      const unsigned u0 = (K == 8 ? (c.jj & 1) * 6 : 8 - (c.jj & 1) * 8) + NUN * c.hf;
#pragma unroll
      for (int t = 0; t < NUN; ++t) cf[t] = ldsr<v2u>(L_CSB + ((u0 + t) * 16 + r15) * 8);
#pragma unroll
      for (int mi = 0; mi < 2; ++mi) nm[mi] = ldsr<v2u>(L_STM + (32 * c.m + 16 * mi + r15) * 8);
    }
#pragma unroll
    for (int i = 0; i < NM; ++i) {
      mm(i);
      rd(2 * i);
      rd(2 * i + 1);
      if (K > 0 && i % (NM / 4) == 1) issue_rt<T, K + 3>(c, i / (NM / 4));
      SCHED();
    }
    if constexpr (EP) {
      const unsigned ob = lane * 16 + WS_A2 + ((2 * c.ti + (c.m >> 1)) * 72 + 9 * c.jj) * 4096 + (c.m & 1) * 2048;
#pragma unroll
      for (int mi = 0; mi < 2; ++mi) {
        if (g) nm[mi] = v2u{0, 0};
        v2u hp[NUN];
#pragma unroll
        for (int t = 0; t < NUN; ++t) {
          const v2u pk = pk4(mfma16(cf[t], nm[mi], K == 8 ? c.acc[t][mi] : c.acc1[t][mi]));
          hp[t] = v2u{relu2(pk.x), relu2(pk.y)};
        }
        if constexpr (K == 8) {
          const unsigned o = ob + 2 * c.hf * 4096 + mi * 1024;
          __builtin_amdgcn_raw_buffer_store_b128(cat(hp[0], hp[1]), c.rws, o, 0, 16);
          __builtin_amdgcn_raw_buffer_store_b128(cat(hp[2], hp[3]), c.rws, o + 4096, 0, 16);
        } else {
          const unsigned o = ob + 4 * 4096 + mi * 1024;
          __builtin_amdgcn_raw_buffer_store_b128(cat(hp[0], hp[1]), c.rws, o + c.hf * 4096, 0, 16);
          __builtin_amdgcn_raw_buffer_store_b64(hp[2], c.rws, o + 8192 + c.hf * 8, 0, 16);
        }
      }
      SCHED();
    }
  }
  if constexpr (!T && K <= 12) {
    auto mm1 = [&](int kc, int i) {
      const v4u wf = cat(c.wb[kc & 1][i][0], c.wb[kc & 1][i][1]), xf = c.X[kc][i >> 1];
      if (i & 1) c.vv = mfmaB(xf, wf, kc == 0 && i < 2 ? zero : c.vv);
      else c.kk = mfmaB(wf, xf, kc == 0 && i < 2 ? zero : c.kk);
    };
    auto mm2 = [&](int kc, int i) { c.qq = mfmaB(cat(c.wb[kc & 1][i][0], c.wb[kc & 1][i][1]), c.X[kc][i], c.qq); };
    if constexpr (K < 8) {
      auto rd1 = [&](int i) {
        if (i < 4) c.X[K & 7][i] = ldsr<v4u>(c.ax[i] + sl);
        else if (i < 20) c.wb[K & 1][(i - 4) >> 1][i & 1] = trrd(c.aw[0] + sl + 16384 + (((i - 4) >> 1) & 1) * 8192 + ((i - 4) >> 2) * 2048 + (i & 1) * 512);
      };
#pragma unroll
      for (int i = 0; i < 8; ++i) {
        if (K > 0) mm1((K - 1) & 7, i);
#pragma unroll
        for (int k = 0; k < 3; ++k) rd1(3 * i + k);
        if (K > 0 && (i & 1)) issue_rt<T, K + 3>(c, i >> 1);
        SCHED();
      }
    } else if constexpr (K == 8) {
      issue<T, K + 3>(c);
      v2u nm = ldsr<v2u>(L_STM + (32 * c.m + r31) * 8);
      const unsigned a = L_CSB + (32 * c.hf + r31) * 8;
      const v2u ck = ldsr<v2u>(a + 512), cv = ldsr<v2u>(a + 1024), cq = ldsr<v2u>(a);
      const float rs = ldsr<float>(L_STR + (32 * c.m + r31) * 4);
      const v4u wu = trfrag(L_WPHI + (2 * c.hf + (lane & 1)) * 512 + (4 * h + (r15 >> 2)) * 32 + (2 * (g & 1) + ((lane >> 1) & 1)) * 8, 256);
      v4u sdf[2];
#pragma unroll
      for (int u = 0; u < 2; ++u) sdf[u] = cat(ldsr<v2u>(L_SDB + (32 * c.m + 16 * u + 4 * h) * 2), ldsr<v2u>(L_SDB + (32 * c.m + 16 * u + 8 + 4 * h) * 2));
#pragma unroll
      for (int i = 0; i < 4; ++i) mm1(7, 2 * i);
      if (h) nm = v2u{0, 0};
      c.kk = mfmaB8(ck, nm, c.kk);
#pragma unroll
      for (int i = 0; i < 4; ++i) mm1(7, 2 * i + 1);
      c.vv = mfmaB8(nm, cv, c.vv);
      const v4u z4 = {0, 0, 0, 0};
      c.wp0 = (lane & 4) ? z4 : wu;
      c.wp1 = (lane & 4) ? wu : z4;
      const f16v p = mfmaB(c.wp1, pk8(c.kk, 8), mfmaB(c.wp0, pk8(c.kk, 0), zero));
      c.qq = mfmaB8(cq, nm, zero);
      const v4u vb0 = pk8(c.vv, 0), vb1 = pk8(c.vv, 8);
      v4u ph[4];
      dual_softmax(p, rs, rs, ph);
      const unsigned tb = 32768u + w * 4096;
#pragma unroll
      for (int t = 0; t < 4; ++t) ldsw<v4u>(tb + r31 * 128 + ((((t >> 1) * 4 + h * 2 + (t & 1)) ^ (r31 & 7)) << 4), ph[t]);
      v4u tf[2][2];
      {
        const unsigned row = 4 * h + (r15 >> 2), ch = (g & 1) * 2 + ((lane >> 1) & 1);
#pragma unroll
        for (int sg = 0; sg < 2; ++sg)
#pragma unroll
          for (int u = 0; u < 2; ++u) tf[sg][u] = trfrag(tb + (row + 16 * u) * 128 + (((sg * 4 + ch) ^ row) << 4) + (lane & 1) * 8, 1024);
      }
#pragma unroll
      for (int sg = 0; sg < 2; ++sg) {
        const f16v kv = mfmaB(tf[sg][1], vb1, mfmaB(tf[sg][0], vb0, zero));
        const f16v zz = mfmaB(sdf[1], tf[sg][1], mfmaB(sdf[0], tf[sg][0], zero));
        const f8v o = (lane & 16) ? __builtin_shufflevector(kv, kv, 8, 9, 10, 11, 12, 13, 14, 15) : __builtin_shufflevector(kv, kv, 0, 1, 2, 3, 4, 5, 6, 7);
        ldsw<f4>(tb + sg * 2048 + lane * 16, __builtin_shufflevector(o, o, 0, 1, 2, 3));
        ldsw<f4>(tb + sg * 2048 + 1024 + lane * 16, __builtin_shufflevector(o, o, 4, 5, 6, 7));
        ldsw<float>(L_ZRED + ((c.hf * 2 + sg) * 4 + c.m) * 128 + r31 * 4, zz[0]);
      }
      SCHED();
    } else if constexpr (K == 9) {
      const unsigned ra = 32768u + (w & 4) * 4096 + (w & 3) * 1024 + lane * 16;
      const unsigned za = L_ZRED + (w >> 1) * 512 + (16 * (r15 >> 3) + 8 * (w & 1) + (r15 & 7)) * 4;
      f4 s = ldsr<f4>(ra);
      float z = ldsr<float>(za);
#pragma unroll
      for (int k = 1; k < 4; ++k) {
        s += ldsr<f4>(ra + k * 4096);
        z += ldsr<float>(za + k * 128);
      }
      __builtin_amdgcn_raw_buffer_store_b64(pk4(s), c.rws, lane * 8 + WS_KV + ((c.jj * 8 + w) * 16 + c.ti) * 512, 0, 16);
      if (lane < 16) __builtin_amdgcn_raw_buffer_store_b32(__float_as_uint(z), c.rws, WS_Z + (c.jj * 8 + w) * 1024 + lane * 64 + c.ti * 4, 0, 16);
      issue<T, K + 3>(c);
    } else if constexpr (K < 12) {
#pragma unroll
      for (int s = 0; s < 4; ++s) {
#pragma unroll
        for (int i = 0; i < 4; ++i) {
          if (K > 10 || s > 0) mm2(4 * (K - 10) + s - 1, i);
          c.wb[s & 1][i][0] = trrd(c.aw[0] + sl + s * 8192 + i * 2048);
          c.wb[s & 1][i][1] = trrd(c.aw[0] + sl + s * 8192 + i * 2048 + 512);
          if (i == 1) issue_rt<T, K + 3>(c, s);
          SCHED();
        }
      }
    } else {
      v2u gk;
      const unsigned ga = WS_FKV + c.jj * 256 + r15 * 8;
      snapshot(c, gk, ga);
#pragma unroll
      for (int i = 0; i < 4; ++i) mm2(7, i);
      dual_softmax(mfmaB(c.wp1, pk8(c.qq, 8), mfmaB(c.wp0, pk8(c.qq, 0), zero)), ldsr<float>(L_STR + (32 * c.m + r31) * 4), 1.f, c.ph);
      asm volatile("" : "+v"(c.ph[0]), "+v"(c.ph[1]), "+v"(c.ph[2]), "+v"(c.ph[3]));
      SCHED();
      gate<0>(c, gk, ga);
      TS(4);
      dma<L_GST, WS_Z, true>(c.m1, r15 * 64 + g * 16, c.rws, (c.jj * 8 + w) * 1024);
      issue<T, K + 3>(c);
    }
  }
  if constexpr (K == 0) {
    issue<T, 2>(c);
    const unsigned th = c.ti >> 3;
    const unsigned lo = T && th ? (c.jj & 1) * 2 : 0, cnt = th ? (T ? 6u : 4u) : 8u;
    if (w - lo < cnt) {
      const unsigned q = r15 >> 2;
      const unsigned a = L_GST + (w >> 2) * 8192 + (8 * g + q) * 128 + (lane & 3) * 8 + (((w & 3) ^ ((q & 2) | (g & 1))) << 5);
      const v4u ones = {0x3f803f80u, 0x3f803f80u, 0x3f803f80u, 0x3f803f80u};
      const f4 s = mfma32(ones, trfrag(a + 4096, 512), mfma32(ones, trfrag(a, 512), z4f));
      const unsigned col = T ? 1536 + 224 * c.jj + 16 * ((th ? 8 - (c.jj & 1) * 8 : (c.jj & 1) * 6) + w - lo)
                             : (th ? 0u : 512 + (w >> 2) * 512) + 64 * c.jj + (w & 3) * 16;
      if (lane < 16) __builtin_amdgcn_raw_buffer_store_b64(v2u{__float_as_uint(s.x), (unsigned)c.tag}, c.rws, WS_CS + (col + lane) * 64 + (c.ti & 7) * 8, 0, 16);
    }
    issue<T, 3>(c);
  }
  if constexpr (K == 7) { if (!c.hf) gjob<T>(c); }
  if constexpr (!T && K == 17) {
    const bool on = (r31 >> 4) == h;
    const unsigned ka = on ? L_KVB + (c.hf * 128 + r31) * 16 : L_ZERO, za = on ? L_ZB + c.hf * 128 + h * 16 : L_ZERO;
    f16v num = zero, den = zero;
#pragma unroll
    for (int t = 0; t < 4; ++t) {
      num = mfmaB(ldsr<v4u>(ka + t * 512), c.ph[t], num);
      den = mfmaB(ldsr<v4u>(za + t * 32), c.ph[t], den);
    }
    const float sd = ldsr<float>(L_STD + (32 * c.m + r31) * 4);
    const float r0 = __builtin_amdgcn_rcpf(den[0] + 1e-6f) * sd, r1 = __builtin_amdgcn_rcpf(den[8] + 1e-6f) * sd;
#pragma unroll
    for (int r = 0; r < 16; ++r) num[r] *= r < 8 ? r0 : r1;
    const unsigned ob = WS_A2 + ((2 * c.ti + (c.m >> 1)) * 72 + 9 * c.jj + 7 + c.hf) * 4096 + (2 * (c.m & 1) + (r31 >> 4)) * 1024 + (r15 + 16 * h) * 16;
    __builtin_amdgcn_raw_buffer_store_b128(pk8(num, 0), c.rws, ob, 0, 16);
    __builtin_amdgcn_raw_buffer_store_b128(pk8(num, 8), c.rws, ob + 512, 0, 16);
    SCHED();
  }
  if constexpr (it.kind == WW || it.kind == AA || it.kind == KV) {
    constexpr bool own_issue = K > 12;
    constexpr int pn = pend<T>(K);
#pragma unroll
    for (int i = 0; i < 16; ++i) {
      if constexpr (pn >= 0) c.acc2[(i >> 1) & 3][i & 1] = mfma32(c.b2[2 * pn + (i >> 3)][i & 1], c.af[pn & 1][i >> 1], pn == 0 && i < 8 ? z4f : c.acc2[(i >> 1) & 3][i & 1]);
      if constexpr (it.kind == WW) { if (i < 4) c.b2[2 * it.n + (i >> 1)][i & 1] = trfrag(c.ab2[i & 1] + sl + (i >> 1) * 4096, 2048); }
      if constexpr (it.kind == AA) { if (i < 8) c.af[it.n & 1][i] = ldsr<v4u>(c.aa[i & 3] + sl + (i >> 2) * 4096); }
      if constexpr (own_issue) { if ((i & 3) == 1) issue_rt<T, K + 3>(c, i >> 2); }
      SCHED();
    }
    if constexpr (it.kind == KV) {
      const unsigned q = r15 >> 2, a = sla + w * 4096 + (4 * g + q) * 256 + (lane & 3) * 8;
      f4 s = it.n ? c.kvs : z4f;
#pragma unroll
      for (int b = 0; b < 8; ++b) {
        const unsigned sel = r15 == (unsigned)(8 * it.n + b) ? 0x3f803f80u : 0u;
        s = mfma16(trrd(a + (((b & 3) ^ q) << 5) + (b >> 2) * 128), v2u{sel, sel}, s);
      }
      c.kvs = s;
      if constexpr (it.n == 1) {
        const f4 zp = ldsr<f4>(L_GST + w * 1024 + lane * 16);
        const float z = xg_add((zp.x + zp.y) + (zp.z + zp.w));
        const unsigned L = 4 * r15 + g;
        ldsw<v2u>(L_KVB + (w * 32 + (L & 31)) * 16 + (L >> 5) * 8, pk4(s));
        if (lane < 16) ldsw<uint16_t>(L_ZB + w * 32 + lane * 2, (uint16_t)pk2(z, 0.f));
      }
    }
  }
}

template <int T, int K> DI void windows(St& c) {
  window<T, K>(c);
  if constexpr (K + 1 < nitems<T>()) windows<T, K + 1>(c);
}

template <int T> DI void program(St& c, const uint16_t* wphi, uint16_t* out) {
  constexpr unsigned WSTR = T ? 3584u : 3072u;
  const unsigned lane = c.lane, w = c.w, g = c.g, r15 = c.r15;
  u64* ts = c.ts;
  const unsigned srow = c.ti * 128 + (T * 8 + c.jj) * 8 + w;
  v4u xrow;
  asm volatile("buffer_load_dwordx4 %0, %1, %2, %3 offen" : "=v"(xrow) : "v"(c.vl), "s"(c.rx), "s"(srow * 1024) : "memory");
  c.vx0 = (c.ti * 128 + 16 * w + (lane >> 3)) * 1024u + (((lane & 7) ^ (lane >> 4)) << 4);
  c.vx1 = (c.vx0 ^ 64u) + 8192u;
  c.vw = (8 * w + (lane >> 3)) * WSTR + (T ? ((448u * c.jj) & ~127u) : c.jj * 128u) + (((lane & 7) ^ ((((lane >> 3) & 2) | (w & 1)) << 1)) << 4);
  c.sp1 = T ? (c.jj & 1) * 256 : 1024u;
  c.sp2 = T ? 256 - (c.jj & 1) * 256 : 0u;
  issue<T, 0>(c);
  {
    const unsigned th = c.ti >> 3, so = (c.ti & 7) * 64 * WSTR + (th ? c.sp2 : c.sp1);
    dma<L_GST, 0>(c.m1, c.vw, c.rw, so);
    dma<L_GST + 8192, 0>(c.m1, c.vw, c.rw, so + (T ? 128u : (th ? 0u : 1024u)));
  }
  issue<T, 1>(c);
  TS(11);
  asm volatile("s_waitcnt vmcnt(10)" : "+v"(xrow)::"memory");
  {
    float v[8];
#pragma unroll
    for (int d = 0; d < 4; ++d) { v[2 * d] = blo(xrow[d]); v[2 * d + 1] = bhi(xrow[d]); }
    const float mean = wave_add(((v[0] + v[1]) + (v[2] + v[3])) + ((v[4] + v[5]) + (v[6] + v[7]))) * (1.f / 512.f);
    float q = 0.f;
#pragma unroll
    for (int d = 0; d < 8; ++d) q = __builtin_fmaf(v[d] - mean, v[d] - mean, q);
    const float var = wave_add(q) * (1.f / 512.f);
    const float sd = __builtin_amdgcn_sqrtf(rbf(rbf(var) + 1e-5f));
    if (lane == 0) __builtin_amdgcn_raw_buffer_store_b64(v2u{pk2(mean, sd), (unsigned)c.tag}, c.rws, WS_ST + srow * 8, 0, 16);
  }
  const unsigned q = r15 >> 2;
  if (T) {
    const unsigned f = (q & 2) | (g & 1), rowp = (8 * g + q) * 128 + (lane & 3) * 8;
#pragma unroll
    for (int ks = 0; ks < 2; ++ks) c.ax[ks] = (4 * c.m + (r15 >> 3)) * 1024 + (r15 & 7) * 128 + (((4 * ks + g) ^ (r15 >> 1)) << 4);
#pragma unroll
    for (int t = 0; t < 4; ++t) c.aw[t] = 16384 + c.hf * 8192 + rowp + ((t ^ f) << 5);
    asm volatile("" : "+v"(c.ax[0]), "+v"(c.ax[1]), "+v"(c.aw[0]), "+v"(c.aw[1]), "+v"(c.aw[2]), "+v"(c.aw[3]));
  } else {
    const unsigned r31 = lane & 31, h = lane >> 5;
#pragma unroll
    for (int u = 0; u < 4; ++u) c.ax[u] = (4 * c.m + (r31 >> 3)) * 1024 + (r31 & 7) * 128 + (((2 * u + h) ^ ((r31 >> 1) & 7)) << 4);
    c.aw[0] = (8 * h + q) * 128 + (lane & 3) * 8 + (((2 * c.hf + (g & 1)) ^ ((q & 2) | h)) << 5);
    asm volatile("" : "+v"(c.ax[0]), "+v"(c.ax[1]), "+v"(c.ax[2]), "+v"(c.ax[3]), "+v"(c.aw[0]));
    ldsw<unsigned>(L_ZERO + threadIdx.x * 4, 0u);
  }

  windows<T, 0>(c);

  const unsigned nh = w & 1, kq = w >> 1;
#pragma unroll
  for (int i = 0; i < 16; ++i) c.acc2[(i >> 1) & 3][i & 1] = mfma32(c.b2[16 + (i >> 3)][i & 1], c.af[0][i >> 1], c.acc2[(i >> 1) & 3][i & 1]);
  TS(9);
  if (blockIdx.x == 0 && threadIdx.x == 0) set_flag(c.ws, WS_EPOCH, c.tag);
  constexpr unsigned rb = (((nitems<T>() - 1) & 3) < 2) ? 65536u : 0u;
#pragma unroll
  for (int s = 1; s < 4; ++s)
#pragma unroll
    for (int nf = 0; nf < 2; ++nf) ldsw<f4>(rb + ((((kq ^ s) * 2 + nh) * 3 + (s - 1)) * 2 + nf) * 1024 + lane * 16, c.acc2[s][nf]);
  WAIT_LGKM0();
  BARRIER();
  const float rs = __builtin_amdgcn_rcpf(bhi(c.gst.x));
  v2u o[2];
#pragma unroll
  for (int nf = 0; nf < 2; ++nf) {
    f4 s = c.acc2[0][nf];
#pragma unroll
    for (int k = 0; k < 3; ++k) s += ldsr<f4>(rb + ((w * 3 + k) * 2 + nf) * 1024 + lane * 16);
    o[nf] = pk4(f4{__builtin_fmaf(s.x, rs, blo(c.xr[nf].x)), __builtin_fmaf(s.y, rs, bhi(c.xr[nf].x)), __builtin_fmaf(s.z, rs, blo(c.xr[nf].y)),
                   __builtin_fmaf(s.w, rs, bhi(c.xr[nf].y))});
  }
  auto sx = __builtin_amdgcn_permlane16_swap(o[0].x, o[1].x, false, false);
  auto sy = __builtin_amdgcn_permlane16_swap(o[0].y, o[1].y, false, false);
  const unsigned col = c.c2 * 64 + nh * 32 + (g & 1) * 16 + (g >> 1) * 8;
  *(v4u*)(out + (size_t)(c.r2 * 64 + kq * 16 + r15) * 512 + col) = v4u{sx[0], sy[0], sx[1], sy[1]};
}

__global__ __launch_bounds__(512, 2) void fused_block(const uint16_t* __restrict__ x, const uint16_t* __restrict__ wqkv,
                                                      const uint16_t* __restrict__ wphi, const uint16_t* __restrict__ wo,
                                                      const uint16_t* __restrict__ w1, const uint16_t* __restrict__ w2,
                                                      uint16_t* __restrict__ out, char* __restrict__ ws) {
  __shared__ __attribute__((aligned(1024))) char smem[LDS_BYTES];
  asm volatile("" ::"s"((unsigned)(uintptr_t)(LAS char*)smem));
  u64 ts[16];
#pragma unroll
  for (int i = 0; i < 16; ++i) ts[i] = 0;
  TS(0);
  St c;
  c.ts = ts;
  const unsigned b = blockIdx.x, xcd = b & 7, idx = b >> 3;
  c.lane = threadIdx.x & 63;
  c.w = rfl(threadIdx.x >> 6);
  c.m = c.w & 3;
  c.hf = c.w >> 2;
  c.g = c.lane >> 4;
  c.r15 = c.lane & 15;
  c.ti = (xcd & 1) * 8 + (idx >> 2);
  const unsigned type = (idx >> 1) & 1;
  c.jj = (xcd >> 1) * 2 + (idx & 1);
  c.r2 = xcd * 4 + (idx >> 3);
  c.c2 = idx & 7;
  c.m1 = c.w * 1024;
  c.m2 = c.w * 2048;
  c.m4 = c.w * 4096;
  c.sw1 = (224 * c.w + 96 * (c.w & 1)) * 1024;
  c.sw2 = (224 * c.w + 128 - 128 * (c.w & 1)) * 1024;
  c.swo = c.w * 65536;
  c.sa2 = WS_A2 + (c.r2 * 72 + 9 * c.w) * 4096;
  c.skv = WS_KV + (c.jj * 8 + c.w) * 8192;
  c.rx = mk_rsrc(x, 2048u * 1024u);
  c.rw2 = mk_rsrc(w2, 1792u * 1024u);
  c.rwo = mk_rsrc(wo, 512u * 1024u);
  c.rws = mk_rsrc(ws, WS_END);
  c.ws = ws;
  c.vl = c.lane * 16;
  c.vb2 = (c.lane >> 3) * 1024u + c.c2 * 128u + (((c.lane & 7) ^ (((c.lane >> 4) & 3) << 1)) << 4);
  c.tag = *(const __attribute__((address_space(4))) u64*)(uintptr_t)(ws + WS_EPOCH) + 1;
  if (type) {
    c.rw = mk_rsrc(w1, 512u * 3584u);
    program<1>(c, wphi, out);
  } else {
    c.rw = mk_rsrc(wqkv, 512u * 3072u);
    if (c.w < 2) dma<L_WPHI, 0>(c.m1, c.vl, mk_rsrc(wphi, 32u * 512u), c.jj * 2048 + c.m1);
    program<0>(c, wphi, out);
  }
}

extern "C" void kernel_launch(void* const* d_in, const int* in_sizes, int n_in, void* d_out, int out_size, void* d_ws,
                              size_t ws_size, hipStream_t stream) {
  fused_block<<<256, 512, 0, stream>>>((const uint16_t*)d_in[0], (const uint16_t*)d_in[1], (const uint16_t*)d_in[2],
                                       (const uint16_t*)d_in[3], (const uint16_t*)d_in[5], (const uint16_t*)d_in[6],
                                       (uint16_t*)d_out, (char*)d_ws);
}
```

```cpp
#include <hip/hip_runtime.h>
#include <stdint.h>

typedef unsigned v4u __attribute__((ext_vector_type(4)));
typedef unsigned v2u __attribute__((ext_vector_type(2)));
typedef float f4 __attribute__((ext_vector_type(4)));
typedef float f2 __attribute__((ext_vector_type(2)));
typedef float f8v __attribute__((ext_vector_type(8)));
typedef float f16v __attribute__((ext_vector_type(16)));
typedef short s8 __attribute__((ext_vector_type(8)));
typedef short s4 __attribute__((ext_vector_type(4)));
typedef short s2 __attribute__((ext_vector_type(2)));
typedef __bf16 bf2 __attribute__((ext_vector_type(2)));
typedef unsigned long long u64;
#define GAS __attribute__((address_space(1)))
#define LAS __attribute__((address_space(3)))
#define DI __device__ __forceinline__
#define RLX_AGENT __ATOMIC_RELAXED, __HIP_MEMORY_SCOPE_AGENT

#define PROFILE 0
#define CODE_PF 40960

constexpr unsigned WS_EPOCH = 0;
constexpr unsigned WS_FKV = 4096;
constexpr unsigned WS_FA = 8192;
constexpr unsigned WS_ST = 16384;
constexpr unsigned WS_Z = 65536;
constexpr unsigned WS_CS = 131072;
constexpr unsigned WS_KV = 393216;
constexpr unsigned WS_A2 = WS_KV + (1u << 19);
constexpr unsigned WS_END = WS_A2 + 32u * 72u * 4096u;
constexpr unsigned WS_TS = 16u << 20;

constexpr unsigned L_GST = 131072;
constexpr unsigned L_MISC = 147456;
constexpr unsigned L_CSB = L_MISC;
constexpr unsigned L_STM = L_MISC + 2048;
constexpr unsigned L_STR = L_MISC + 3072;
constexpr unsigned L_STD = L_MISC + 3584;
constexpr unsigned L_WPHI = L_MISC + 4096;
constexpr unsigned L_ZRED = L_MISC + 6144;
constexpr unsigned L_ZERO = L_MISC + 8192;
constexpr unsigned L_KVB = L_MISC + 10240;
constexpr unsigned L_ZB = L_MISC + 14336;
constexpr unsigned L_SDB = L_MISC + 14592;
constexpr unsigned LDS_BYTES = 163840;

typedef __amdgpu_buffer_rsrc_t rsrc_t;
DI rsrc_t mk_rsrc(const void* p, unsigned bytes) { return __builtin_amdgcn_make_buffer_rsrc((void*)p, (short)0, (int)bytes, 0x00020000); }

template <unsigned MC, unsigned SC, bool SC1 = false> DI void dma(unsigned mb, unsigned voff, rsrc_t rs, unsigned sb) {
  unsigned t;
  if constexpr (SC1)
    asm volatile("s_add_u32 m0, %1, %2\n\ts_add_u32 %0, %3, %4\n\tbuffer_load_dwordx4 %5, %6, %0 offen sc1 lds"
                 : "=&s"(t) : "s"(mb), "i"(MC), "s"(sb), "i"(SC), "v"(voff), "s"(rs) : "memory", "scc");
  else
    asm volatile("s_add_u32 m0, %1, %2\n\ts_add_u32 %0, %3, %4\n\tbuffer_load_dwordx4 %5, %6, %0 offen lds"
                 : "=&s"(t) : "s"(mb), "i"(MC), "s"(sb), "i"(SC), "v"(voff), "s"(rs) : "memory", "scc");
}
#define WAIT_VM(n) asm volatile("s_waitcnt vmcnt(%0)" ::"i"(n) : "memory")
#define WAIT_LGKM0() do { __builtin_amdgcn_s_waitcnt(0xC07F); asm volatile("" ::: "memory"); } while (0)
#define BARRIER() asm volatile("s_barrier" ::: "memory")
#define SCHED() __builtin_amdgcn_sched_barrier(0)
#define PIN(x) asm volatile("" : "+v"(x))

DI f4 mfma32(v4u a, v4u b, f4 c) { return __builtin_amdgcn_mfma_f32_16x16x32_bf16(__builtin_bit_cast(s8, a), __builtin_bit_cast(s8, b), c, 0, 0, 0); }
DI f4 mfma16(v2u a, v2u b, f4 c) { return __builtin_amdgcn_mfma_f32_16x16x16bf16_1k(__builtin_bit_cast(s4, a), __builtin_bit_cast(s4, b), c, 0, 0, 0); }
DI f16v mfmaB(v4u a, v4u b, f16v c) { return __builtin_amdgcn_mfma_f32_32x32x16_bf16(__builtin_bit_cast(s8, a), __builtin_bit_cast(s8, b), c, 0, 0, 0); }
DI f16v mfmaB8(v2u a, v2u b, f16v c) { return __builtin_amdgcn_mfma_f32_32x32x8bf16_1k(__builtin_bit_cast(s4, a), __builtin_bit_cast(s4, b), c, 0, 0, 0); }
DI unsigned pk2(float a, float b) { bf2 r = {(__bf16)a, (__bf16)b}; return __builtin_bit_cast(unsigned, r); }
DI v2u pk4(f4 v) { return v2u{pk2(v.x, v.y), pk2(v.z, v.w)}; }
DI v4u pk8(const f16v& v, int o) { return v4u{pk2(v[o], v[o + 1]), pk2(v[o + 2], v[o + 3]), pk2(v[o + 4], v[o + 5]), pk2(v[o + 6], v[o + 7])}; }
DI unsigned relu2(unsigned p) { return __builtin_bit_cast(unsigned, __builtin_elementwise_max(__builtin_bit_cast(s2, p), s2{0, 0})); }
DI float rbf(float a) { return __uint_as_float(pk2(a, 0.f) << 16); }
DI float blo(unsigned p) { return __uint_as_float(p << 16); }
DI float bhi(unsigned p) { return __uint_as_float(p & 0xffff0000u); }
DI v2u trrd(unsigned addr) { return __builtin_bit_cast(v2u, __builtin_amdgcn_ds_read_tr16_b64_v4i16((LAS s4*)(uintptr_t)addr)); }
DI v4u cat(v2u a, v2u b) { return v4u{a.x, a.y, b.x, b.y}; }
DI v4u trfrag(unsigned addr, unsigned off2) { return cat(trrd(addr), trrd(addr + off2)); }
template <class T> DI T ldsr(unsigned addr) { return *(LAS T*)(uintptr_t)addr; }
template <class T> DI void ldsw(unsigned addr, T v) { *(LAS T*)(uintptr_t)addr = v; }
DI unsigned rfl(unsigned x) { return __builtin_amdgcn_readfirstlane(x); }
DI float max3(float a, float b, float c) { return __builtin_elementwise_maximum(__builtin_elementwise_maximum(a, b), c); }
DI float min3(float a, float b, float c) { return __builtin_elementwise_minimum(__builtin_elementwise_minimum(a, b), c); }
template <int CTRL> DI float dpp_add(float x) { return x + __builtin_bit_cast(float, __builtin_amdgcn_update_dpp(0, __builtin_bit_cast(int, x), CTRL, 0xf, 0xf, false)); }

DI float xg_add(float x) {
  auto a = __builtin_amdgcn_permlane16_swap(__float_as_uint(x), __float_as_uint(x), false, false);
  x = __uint_as_float(a[0]) + __uint_as_float(a[1]);
  auto b = __builtin_amdgcn_permlane32_swap(__float_as_uint(x), __float_as_uint(x), false, false);
  return __uint_as_float(b[0]) + __uint_as_float(b[1]);
}
DI float wave_add(float x) { return xg_add(dpp_add<0x121>(dpp_add<0x122>(dpp_add<0x124>(dpp_add<0x128>(x))))); }
DI float sum16(const f16v& v) {
  const f8v a = __builtin_shufflevector(v, v, 0, 1, 2, 3, 4, 5, 6, 7) + __builtin_shufflevector(v, v, 8, 9, 10, 11, 12, 13, 14, 15);
  const f4 b = __builtin_shufflevector(a, a, 0, 1, 2, 3) + __builtin_shufflevector(a, a, 4, 5, 6, 7);
  const f2 c = __builtin_shufflevector(b, b, 0, 1) + __builtin_shufflevector(b, b, 2, 3);
  return c.x + c.y;
}
DI float max16(const f16v& p) {
  return max3(max3(max3(p[0], p[1], p[2]), max3(p[3], p[4], p[5]), max3(p[6], p[7], p[8])),
              max3(max3(p[9], p[10], p[11]), max3(p[12], p[13], p[14]), p[15]), p[15]);
}
DI float min16(const f16v& p) {
  return min3(min3(min3(p[0], p[1], p[2]), min3(p[3], p[4], p[5]), min3(p[6], p[7], p[8])),
              min3(min3(p[9], p[10], p[11]), min3(p[12], p[13], p[14]), p[15]), p[15]);
}
DI void dual_softmax(const f16v& p, float rs, float os, v4u (&o)[4]) {
  const float sc = rs * 1.4426950408889634f;
  const float a = -max16(p) * sc, b = min16(p) * sc;
  f16v e, n;
#pragma unroll
  for (int r = 0; r < 16; ++r) {
    e[r] = __builtin_amdgcn_exp2f(__builtin_fmaf(p[r], sc, a));
    n[r] = __builtin_amdgcn_exp2f(__builtin_fmaf(p[r], -sc, b));
  }
  e *= __builtin_amdgcn_rcpf(sum16(e)) * os;
  n *= __builtin_amdgcn_rcpf(sum16(n)) * os;
  o[0] = pk8(e, 0);
  o[1] = pk8(e, 8);
  o[2] = pk8(n, 0);
  o[3] = pk8(n, 8);
}

constexpr unsigned SPIN_CAP = 1u << 18;
DI void set_flag(char* ws, unsigned off, u64 tag) { __hip_atomic_store((GAS u64*)(ws + off), tag, RLX_AGENT); }

#define TS(k)

enum Kind { P1, P2, WW, AA, KV, RED, END };
struct Item { int kind, n; };
#define SEQ_B {WW,0},{WW,1},{WW,2},{WW,3},{WW,4},{AA,0},{WW,5},{AA,1},{WW,6},{AA,2},{WW,7},{AA,3},{WW,8},{AA,4},{AA,5},{AA,6},{AA,7},{AA,8}
#define SEQ_A {AA,0},{WW,4},{AA,1},{WW,5},{AA,2},{WW,6},{AA,3},{WW,7},{AA,4},{WW,8},{AA,5},{AA,6},{AA,7},{AA,8}
constexpr Item seqB[] = {{P1,0},{P1,1},{P1,2},{P1,3},{P1,4},{P1,5},{P1,6},{P1,7},{P2,0},{P2,1},{P2,2},{P2,3}, SEQ_B};
constexpr Item seqA[] = {{P1,0},{P1,1},{P1,2},{P1,3},{P1,4},{P1,5},{P1,6},{P1,7},{WW,0},{RED,0},{P2,0},{P2,1},{WW,1},{WW,2},{WW,3},{KV,0},{KV,1}, SEQ_A};
template <int T> constexpr int nitems() { return T ? (int)(sizeof(seqB) / sizeof(Item)) : (int)(sizeof(seqA) / sizeof(Item)); }
template <int T> constexpr Item item_at(int k) { return k < 0 || k >= nitems<T>() ? Item{END, 0} : (T ? seqB[k] : seqA[k]); }
template <int T> constexpr int nops(int k) { return item_at<T>(k).kind == END || item_at<T>(k).kind == RED ? 0 : 4; }
template <int T> constexpr int gate_of(int k) {
  const Item it = item_at<T>(k);
  return it.kind == AA ? (it.n == 0 ? 0 : (it.n == 4 ? 1 : (it.n == 7 ? 2 : -1))) : -1;
}
template <int T> constexpr int pend(int k) { return item_at<T>(k - 1).kind == AA ? item_at<T>(k - 1).n : -1; }
template <int T> constexpr int lenient(int K) {
  if (K == 0) return -3;
  if (K == 1) return 1;
  if (T) return (K == 9 || K == 10 || (K >= 13 && K <= 15)) ? 4 : 0;
  return (K == 10 || K == 18) ? 2 : 0;
}

struct St {
  rsrc_t rx, rw, rw2, rwo, rws;
  char* ws;
  u64 tag;
  unsigned lane, w, m, hf, g, r15, ti, jj, r2, c2;
  unsigned m1, m2, m4;
  unsigned sp1, sp2, sw1, sw2, swo, sa2, skv;
  unsigned vx0, vx1, vw, vb2, vl;
  unsigned ax[4], aw[4], aw2[3], ab2[2], aa[4];
  v4u X[8][4];
  v2u wb[2][12][2];
  f4 acc[4][2], acc1[3][2];
  f16v kk, vv, qq;
  v4u wp0, wp1, ph[4];
  f4 kvs;
  v4u b2[18][2], af[2][8];
  f4 acc2[4][2];
  v2u gate[3], xr[2], gst;
  u64* ts;
};

template <int T, int K, int p> DI void issue_piece(St& c) {
  constexpr Item it = item_at<T>(K);
  constexpr unsigned sl = (K & 3) * 32768u, WSTR = T ? 3584u : 3072u, n = it.n;
  if constexpr (it.kind == P1) {
    if constexpr (p < 2) dma<sl + p * 1024, n * 128>(c.m2, p ? c.vx1 : c.vx0, c.rx, 0);
    else dma<sl + 16384 + (p - 2) * 8192, n * 64 * WSTR + (p - 2) * (T ? 128 : 1024)>(c.m1, c.vw, c.rw, c.sp1);
  } else if constexpr (it.kind == P2) {
    dma<sl + p * 8192, (T ? 2 * n + (p >> 1) : 4 * n + p) * 64 * WSTR + (T ? (p & 1) * 128 : 0)>(c.m1, c.vw, c.rw, c.sp2);
  } else if constexpr (it.kind == WW) {
    if constexpr (n < 4) dma<sl + p * 1024, (32 * n + 8 * p) * 1024>(c.m4, c.vb2, c.rw2, c.sw1);
    else if constexpr (n < 7) dma<sl + p * 1024, (16 * (p < 2 ? (n == 4 ? 0 : (n == 5 ? 3 : 2)) : (n == 4 ? 1 : (n == 5 ? 4 : 5))) + 8 * (p & 1)) * 1024>(c.m4, c.vb2, c.rw2, c.sw2);
    else dma<sl + p * 1024, (32 * (n - 7) + 16 * (p & 1) + 8 * (p >> 1)) * 1024>(c.m4, c.vb2, c.rwo, c.swo);
  } else if constexpr (it.kind == AA) {
    dma<sl + p * 1024, n * 4096 + p * 1024, true>(c.m4, c.vl, c.rws, c.sa2);
  } else if constexpr (it.kind == KV) {
    dma<sl + p * 1024, n * 256 + p * 2048, true>(c.m4, c.g * 512 + ((c.r15 ^ (c.g << 1)) << 4), c.rws, c.skv);
  }
}
template <int T, int K> DI void issue_rt(St& c, int p) {
  if (p == 0) issue_piece<T, K, 0>(c);
  if (p == 1) issue_piece<T, K, 1>(c);
  if (p == 2) issue_piece<T, K, 2>(c);
  if (p == 3) issue_piece<T, K, 3>(c);
}
template <int T, int K> DI void issue(St& c) {
  issue_piece<T, K, 0>(c);
  issue_piece<T, K, 1>(c);
  issue_piece<T, K, 2>(c);
  issue_piece<T, K, 3>(c);
}
DI void snapshot(St& c, v2u& r, unsigned addr) { asm volatile("buffer_load_dwordx2 %0, %1, %2, 0 offen sc1" : "=v"(r) : "v"(addr), "s"(c.rws) : "memory"); }
template <int N> DI void gate(St& c, v2u& r, unsigned addr) {
  asm volatile("s_waitcnt vmcnt(%1)" : "+v"(r) : "i"(N) : "memory");
  if (!__all(r.x == (unsigned)c.tag && r.y == (unsigned)(c.tag >> 32))) {
    GAS u64* p = (GAS u64*)(c.ws + addr);
#pragma unroll 1
    for (unsigned spins = 0; spins < SPIN_CAP; ++spins) {
      if (__all(__hip_atomic_load(p, RLX_AGENT) == c.tag)) break;
      __builtin_amdgcn_s_sleep(2);
    }
  }
  asm volatile("" ::: "memory");
}
template <int T> DI void gload(St& c) {
  const unsigned col = T ? 1536 + 224 * c.jj + 32 * c.w : (c.w < 6 ? c.w >> 1 : 2u) * 512 + 64 * c.jj + 32 * (c.w & 1);
  dma<L_GST, WS_CS, true>(c.m2, c.vl, c.rws, col * 64);
  dma<L_GST + 1024, WS_CS + 1024, true>(c.m2, c.vl, c.rws, col * 64);
  if (c.w < 2) dma<L_ZRED, WS_ST, true>(c.m1, c.vl, c.rws, c.ti * 1024);
}
template <int T> DI void gjob(St& c) {
  const unsigned lane = c.lane, w = c.w, tg = (unsigned)c.tag;
  v4u g0, g1;
  v2u gs;
#pragma unroll 1
  for (unsigned spins = 0; spins < SPIN_CAP; ++spins) {
    g0 = ldsr<v4u>(L_GST + threadIdx.x * 32);
    g1 = ldsr<v4u>(L_GST + threadIdx.x * 32 + 16);
    gs = ldsr<v2u>(L_ZRED + (w & 1) * 1024 + (threadIdx.x & 127) * 8);
    const bool ok = ((g0.y == tg && g0.w == tg && g1.y == tg && g1.w == tg) || (T && w == 7)) && (gs.y == tg || w >= 2);
    if (__all(ok)) break;
    gload<T>(c);
    WAIT_VM(0);
  }
  const float s = dpp_add<0xB1>((__uint_as_float(g0.x) + __uint_as_float(g0.z)) + (__uint_as_float(g1.x) + __uint_as_float(g1.z)));
  const float hi = rbf(s), mid = rbf(s - hi);
  if (!(lane & 1)) ldsw<v2u>(L_CSB + (threadIdx.x >> 1) * 8, v2u{pk2(hi, mid), pk2((s - hi) - mid, 0.f)});
  if (w < 2) {
    const unsigned nm = (gs.x & 0xffffu) ^ 0x8000u;
    ldsw<v2u>(L_STM + threadIdx.x * 8, v2u{nm | (nm << 16), nm});
    if (!T) {
      ldsw<float>(L_STR + threadIdx.x * 4, __builtin_amdgcn_rcpf(bhi(gs.x)));
      ldsw<float>(L_STD + threadIdx.x * 4, bhi(gs.x));
      ldsw<uint16_t>(L_SDB + threadIdx.x * 2, (uint16_t)(gs.x >> 16));
    }
  }
  SCHED();
}

template <int T, int K> DI void window(St& c) {
  constexpr Item it = item_at<T>(K);
  constexpr unsigned sl = (K & 1) * 32768u;
  constexpr unsigned hi = (K & 2) * 32768u, sla = sl + hi;
  constexpr int base = nops<T>(K + 1) + nops<T>(K + 2);
  constexpr int K2 = T ? 12 : 8;
  const unsigned lane = c.lane, w = c.w, g = c.g, r15 = c.r15, r31 = lane & 31, h = lane >> 5;
  u64* ts = c.ts;
  WAIT_LGKM0();
  WAIT_VM(base + lenient<T>(K));
  BARRIER();
  SCHED();
  if constexpr (K == 0) TS(12);
  if constexpr (K == 8) TS(1);
  if constexpr (K == (T ? 12 : 9)) TS(2);
  if constexpr (it.kind == AA && it.n == 0) TS(7);
  if constexpr (it.kind == AA && it.n == 4) TS(8);
  if constexpr (it.kind == AA && it.n == 7) TS(13);
  if constexpr (it.kind == KV && it.n == 0) TS(5);
  if constexpr (T && K == 11) { if (threadIdx.x == 0) set_flag(c.ws, WS_FA + c.ti * 512 + c.jj * 8, c.tag); TS(3); }
  if constexpr (T && K == 16) { if (threadIdx.x == 0) set_flag(c.ws, WS_FA + c.ti * 512 + 128 + c.jj * 8, c.tag); TS(4); }
  if constexpr (!T && K == 11) { if (threadIdx.x == 0) set_flag(c.ws, WS_FKV + c.jj * 256 + c.ti * 8, c.tag); TS(3); }
  if constexpr (!T && K == 19) { if (threadIdx.x == 0) set_flag(c.ws, WS_FA + c.ti * 512 + 256 + c.jj * 8, c.tag); TS(6); }
  if constexpr (gate_of<T>(K + 3) >= 0) {
    constexpr int gt = gate_of<T>(K + 3);
    gate<base>(c, c.gate[gt], WS_FA + (c.r2 >> 1) * 512 + gt * 128 + w * 8);
    if constexpr (gt == 0 && T) TS(5);
    if constexpr (gt == 1 && T) TS(6);
    if constexpr (gt == 2) TS(14);
  }
  if constexpr (gate_of<T>(K + 5) >= 0) snapshot(c, c.gate[gate_of<T>(K + 5)], WS_FA + (c.r2 >> 1) * 512 + gate_of<T>(K + 5) * 128 + w * 8);
  if constexpr (K == 5) gload<T>(c);
  if constexpr (K == nitems<T>() - 4) {
    const unsigned row = c.r2 * 64 + (w >> 1) * 16 + r15;
    const unsigned xo = row * 1024 + (c.c2 * 64 + (w & 1) * 32 + 4 * g) * 2;
    c.xr[0] = __builtin_amdgcn_raw_buffer_load_b64(c.rx, xo, 0, 0);
    c.xr[1] = __builtin_amdgcn_raw_buffer_load_b64(c.rx, xo + 32, 0, 0);
    c.gst = __builtin_amdgcn_raw_buffer_load_b64(c.rws, WS_ST + row * 8, 0, 16);
    asm volatile("" ::: "memory");
  }
  if constexpr (K > 0 && !(K & 1)) {
    if constexpr (K < 8) {
#pragma unroll
      for (int t = 0; t < (T ? 2 : 4); ++t) { c.ax[t] ^= 65536u; PIN(c.ax[t]); }
    }
    if constexpr (K < (T ? 8 : 12)) {
#pragma unroll
      for (int t = 0; t < (T ? 4 : 1); ++t) { c.aw[t] ^= 65536u; PIN(c.aw[t]); }
    }
    if constexpr (T && K == 10) {
#pragma unroll
      for (int t = 0; t < 3; ++t) { c.aw2[t] ^= 65536u; PIN(c.aw2[t]); }
    }
    if constexpr (K > K2) {
#pragma unroll
      for (int t = 0; t < 2; ++t) { c.ab2[t] ^= 65536u; PIN(c.ab2[t]); }
    }
    if constexpr (K > 16) {
#pragma unroll
      for (int t = 0; t < 4; ++t) { c.aa[t] ^= 65536u; PIN(c.aa[t]); }
    }
  }
  if constexpr (T && K == 8) {
    const unsigned q = r15 >> 2;
#pragma unroll
    for (int t = 0; t < 3; ++t) {
      const unsigned lu = (c.jj & 1) * 2 + 3 * c.hf + t;
      c.aw2[t] = (lu >> 2) * 8192 + (8 * g + q) * 128 + (lane & 3) * 8 + (((lu & 3) ^ ((q & 2) | (g & 1))) << 5);
    }
  }
  if constexpr (K == K2) {
    const unsigned q = r15 >> 2;
#pragma unroll
    for (int nf = 0; nf < 2; ++nf) c.ab2[nf] = (w >> 1) * 8192 + (4 * g + q) * 128 + (((2 * (w & 1) + nf) ^ ((q >> 1) | ((g & 1) << 1))) << 5) + (lane & 3) * 8;
  }
  if constexpr (K == 16) {
#pragma unroll
    for (int s = 0; s < 4; ++s) c.aa[s] = (w >> 1) * 8192 + ((s ^ (w >> 1)) << 10) + lane * 16;
  }
  static_assert(!(it.kind == AA && K < 16), "aa is formed in window 16");
  const f4 z4f = {0.f, 0.f, 0.f, 0.f};
  f16v zero;
#pragma unroll
  for (int r = 0; r < 16; ++r) zero[r] = 0.f;

  if constexpr (K == 7) { if (c.hf) gjob<T>(c); }
  if constexpr (T && K <= 12) {
    constexpr int cb = K & 1, pb = cb ^ 1;
    auto rd = [&](int i) {
      if constexpr (K < 8) {
        if (i < 4) c.X[K & 7][i] = ldsr<v4u>(c.ax[i >> 1] + sl + (i & 1) * 2048);
        else if (i < 20) c.wb[cb][(i - 4) >> 1][i & 1] = trrd(c.aw[((i - 4) >> 1) & 3] + sl + ((i - 4) >> 3) * 4096 + (i & 1) * 512);
      } else if constexpr (K < 12) {
        if (i < 24) c.wb[cb][i >> 1][i & 1] = trrd(c.aw2[(i >> 1) % 3] + sl + (i / 12) * 16384 + ((i / 6) & 1) * 4096 + (i & 1) * 512);
      }
    };
    constexpr int NM = K <= 8 ? 16 : 24;
    auto mm = [&](int i) {
      const v4u wf = cat(c.wb[pb][i >> 1][0], c.wb[pb][i >> 1][1]);
      if constexpr (K >= 1 && K <= 8) c.acc[(i >> 1) & 3][i & 1] = mfma32(wf, c.X[(K - 1) & 7][2 * (i >> 3) + (i & 1)], K == 1 && i < 8 ? z4f : c.acc[(i >> 1) & 3][i & 1]);
      if constexpr (K > 8) c.acc1[(i >> 1) % 3][i & 1] = mfma32(wf, c.X[(2 * (K - 9) + i / 12) & 7][2 * ((i / 6) & 1) + (i & 1)], K == 9 && i < 6 ? z4f : c.acc1[(i >> 1) % 3][i & 1]);
    };
    constexpr bool EP = K == 8 || K == 12;
    constexpr int NUN = K == 8 ? 4 : 3;
    v2u cf[4], nm[2];
    if constexpr (EP) {
      const unsigned u0 = (K == 8 ? (c.jj & 1) * 6 : 8 - (c.jj & 1) * 8) + NUN * c.hf;
#pragma unroll
      for (int t = 0; t < NUN; ++t) cf[t] = ldsr<v2u>(L_CSB + ((u0 + t) * 16 + r15) * 8);
#pragma unroll
      for (int mi = 0; mi < 2; ++mi) nm[mi] = ldsr<v2u>(L_STM + (32 * c.m + 16 * mi + r15) * 8);
    }
#pragma unroll
    for (int i = 0; i < NM; ++i) {
      mm(i);
      rd(2 * i);
      rd(2 * i + 1);
      if (K > 0 && i % (NM / 4) == 1) issue_rt<T, K + 3>(c, i / (NM / 4));
      SCHED();
    }
    if constexpr (EP) {
      const unsigned ob = lane * 16 + WS_A2 + ((2 * c.ti + (c.m >> 1)) * 72 + 9 * c.jj) * 4096 + (c.m & 1) * 2048;
#pragma unroll
      for (int mi = 0; mi < 2; ++mi) {
        if (g) nm[mi] = v2u{0, 0};
        v2u hp[NUN];
#pragma unroll
        for (int t = 0; t < NUN; ++t) {
          const v2u pk = pk4(mfma16(cf[t], nm[mi], K == 8 ? c.acc[t][mi] : c.acc1[t][mi]));
          hp[t] = v2u{relu2(pk.x), relu2(pk.y)};
        }
        if constexpr (K == 8) {
          const unsigned o = ob + 2 * c.hf * 4096 + mi * 1024;
          __builtin_amdgcn_raw_buffer_store_b128(cat(hp[0], hp[1]), c.rws, o, 0, 16);
          __builtin_amdgcn_raw_buffer_store_b128(cat(hp[2], hp[3]), c.rws, o + 4096, 0, 16);
        } else {
          const unsigned o = ob + 4 * 4096 + mi * 1024;
          __builtin_amdgcn_raw_buffer_store_b128(cat(hp[0], hp[1]), c.rws, o + c.hf * 4096, 0, 16);
          __builtin_amdgcn_raw_buffer_store_b64(hp[2], c.rws, o + 8192 + c.hf * 8, 0, 16);
        }
      }
      SCHED();
    }
  }
  if constexpr (!T && K <= 12) {
    auto mm1 = [&](int kc, int i) {
      const v4u wf = cat(c.wb[kc & 1][i][0], c.wb[kc & 1][i][1]), xf = c.X[kc][i >> 1];
      if (i & 1) c.vv = mfmaB(xf, wf, kc == 0 && i < 2 ? zero : c.vv);
      else c.kk = mfmaB(wf, xf, kc == 0 && i < 2 ? zero : c.kk);
    };
    auto mm2 = [&](int kc, int i) { c.qq = mfmaB(cat(c.wb[kc & 1][i][0], c.wb[kc & 1][i][1]), c.X[kc][i], c.qq); };
    if constexpr (K < 8) {
      auto rd1 = [&](int i) {
        if (i < 4) c.X[K & 7][i] = ldsr<v4u>(c.ax[i] + sl);
        else if (i < 20) c.wb[K & 1][(i - 4) >> 1][i & 1] = trrd(c.aw[0] + sl + 16384 + (((i - 4) >> 1) & 1) * 8192 + ((i - 4) >> 2) * 2048 + (i & 1) * 512);
      };
#pragma unroll
      for (int i = 0; i < 8; ++i) {
        if (K > 0) mm1((K - 1) & 7, i);
#pragma unroll
        for (int k = 0; k < 3; ++k) rd1(3 * i + k);
        if (K > 0 && (i & 1)) issue_rt<T, K + 3>(c, i >> 1);
        SCHED();
      }
    } else if constexpr (K == 8) {
      issue<T, K + 3>(c);
      v2u nm = ldsr<v2u>(L_STM + (32 * c.m + r31) * 8);
      const unsigned a = L_CSB + (32 * c.hf + r31) * 8;
      const v2u ck = ldsr<v2u>(a + 512), cv = ldsr<v2u>(a + 1024), cq = ldsr<v2u>(a);
      const float rs = ldsr<float>(L_STR + (32 * c.m + r31) * 4);
      const v4u wu = trfrag(L_WPHI + (2 * c.hf + (lane & 1)) * 512 + (4 * h + (r15 >> 2)) * 32 + (2 * (g & 1) + ((lane >> 1) & 1)) * 8, 256);
      v4u sdf[2];
#pragma unroll
      for (int u = 0; u < 2; ++u) sdf[u] = cat(ldsr<v2u>(L_SDB + (32 * c.m + 16 * u + 4 * h) * 2), ldsr<v2u>(L_SDB + (32 * c.m + 16 * u + 8 + 4 * h) * 2));
#pragma unroll
      for (int i = 0; i < 4; ++i) mm1(7, 2 * i);
      if (h) nm = v2u{0, 0};
      c.kk = mfmaB8(ck, nm, c.kk);
#pragma unroll
      for (int i = 0; i < 4; ++i) mm1(7, 2 * i + 1);
      c.vv = mfmaB8(nm, cv, c.vv);
      const v4u z4 = {0, 0, 0, 0};
      c.wp0 = (lane & 4) ? z4 : wu;
      c.wp1 = (lane & 4) ? wu : z4;
      const f16v p = mfmaB(c.wp1, pk8(c.kk, 8), mfmaB(c.wp0, pk8(c.kk, 0), zero));
      c.qq = mfmaB8(cq, nm, zero);
      const v4u vb0 = pk8(c.vv, 0), vb1 = pk8(c.vv, 8);
      v4u ph[4];
      dual_softmax(p, rs, rs, ph);
      const unsigned tb = 32768u + w * 4096;
#pragma unroll
      for (int t = 0; t < 4; ++t) ldsw<v4u>(tb + r31 * 128 + ((((t >> 1) * 4 + h * 2 + (t & 1)) ^ (r31 & 7)) << 4), ph[t]);
      v4u tf[2][2];
      {
        const unsigned row = 4 * h + (r15 >> 2), ch = (g & 1) * 2 + ((lane >> 1) & 1);
#pragma unroll
        for (int sg = 0; sg < 2; ++sg)
#pragma unroll
          for (int u = 0; u < 2; ++u) tf[sg][u] = trfrag(tb + (row + 16 * u) * 128 + (((sg * 4 + ch) ^ row) << 4) + (lane & 1) * 8, 1024);
      }
#pragma unroll
      for (int sg = 0; sg < 2; ++sg) {
        const f16v kv = mfmaB(tf[sg][1], vb1, mfmaB(tf[sg][0], vb0, zero));
        const f16v zz = mfmaB(sdf[1], tf[sg][1], mfmaB(sdf[0], tf[sg][0], zero));
        const f8v o = (lane & 16) ? __builtin_shufflevector(kv, kv, 8, 9, 10, 11, 12, 13, 14, 15) : __builtin_shufflevector(kv, kv, 0, 1, 2, 3, 4, 5, 6, 7);
        ldsw<f4>(tb + sg * 2048 + lane * 16, __builtin_shufflevector(o, o, 0, 1, 2, 3));
        ldsw<f4>(tb + sg * 2048 + 1024 + lane * 16, __builtin_shufflevector(o, o, 4, 5, 6, 7));
        ldsw<float>(L_ZRED + ((c.hf * 2 + sg) * 4 + c.m) * 128 + r31 * 4, zz[0]);
      }
      SCHED();
    } else if constexpr (K == 9) {
      const unsigned ra = 32768u + (w & 4) * 4096 + (w & 3) * 1024 + lane * 16;
      const unsigned za = L_ZRED + (w >> 1) * 512 + (16 * (r15 >> 3) + 8 * (w & 1) + (r15 & 7)) * 4;
      f4 s = ldsr<f4>(ra);
      float z = ldsr<float>(za);
#pragma unroll
      for (int k = 1; k < 4; ++k) {
        s += ldsr<f4>(ra + k * 4096);
        z += ldsr<float>(za + k * 128);
      }
      __builtin_amdgcn_raw_buffer_store_b64(pk4(s), c.rws, lane * 8 + WS_KV + ((c.jj * 8 + w) * 16 + c.ti) * 512, 0, 16);
      if (lane < 16) __builtin_amdgcn_raw_buffer_store_b32(__float_as_uint(z), c.rws, WS_Z + (c.jj * 8 + w) * 1024 + lane * 64 + c.ti * 4, 0, 16);
      issue<T, K + 3>(c);
    } else if constexpr (K < 12) {
#pragma unroll
      for (int s = 0; s < 4; ++s) {
#pragma unroll
        for (int i = 0; i < 4; ++i) {
          if (K > 10 || s > 0) mm2(4 * (K - 10) + s - 1, i);
          c.wb[s & 1][i][0] = trrd(c.aw[0] + sl + s * 8192 + i * 2048);
          c.wb[s & 1][i][1] = trrd(c.aw[0] + sl + s * 8192 + i * 2048 + 512);
          if (i == 1) issue_rt<T, K + 3>(c, s);
          SCHED();
        }
      }
    } else {
      v2u gk;
      const unsigned ga = WS_FKV + c.jj * 256 + r15 * 8;
      snapshot(c, gk, ga);
#pragma unroll
      for (int i = 0; i < 4; ++i) mm2(7, i);
      dual_softmax(mfmaB(c.wp1, pk8(c.qq, 8), mfmaB(c.wp0, pk8(c.qq, 0), zero)), ldsr<float>(L_STR + (32 * c.m + r31) * 4), 1.f, c.ph);
      asm volatile("" : "+v"(c.ph[0]), "+v"(c.ph[1]), "+v"(c.ph[2]), "+v"(c.ph[3]));
      SCHED();
      gate<0>(c, gk, ga);
      TS(4);
      dma<L_GST, WS_Z, true>(c.m1, r15 * 64 + g * 16, c.rws, (c.jj * 8 + w) * 1024);
      issue<T, K + 3>(c);
    }
  }
  if constexpr (K == 0) {
    issue<T, 2>(c);
    const unsigned th = c.ti >> 3;
    const unsigned lo = T && th ? (c.jj & 1) * 2 : 0, cnt = th ? (T ? 6u : 4u) : 8u;
    if (w - lo < cnt) {
      const unsigned q = r15 >> 2;
      const unsigned a = L_GST + (w >> 2) * 8192 + (8 * g + q) * 128 + (lane & 3) * 8 + (((w & 3) ^ ((q & 2) | (g & 1))) << 5);
      const v4u ones = {0x3f803f80u, 0x3f803f80u, 0x3f803f80u, 0x3f803f80u};
      const f4 s = mfma32(ones, trfrag(a + 4096, 512), mfma32(ones, trfrag(a, 512), z4f));
      const unsigned col = T ? 1536 + 224 * c.jj + 16 * ((th ? 8 - (c.jj & 1) * 8 : (c.jj & 1) * 6) + w - lo)
                             : (th ? 0u : 512 + (w >> 2) * 512) + 64 * c.jj + (w & 3) * 16;
      if (lane < 16) __builtin_amdgcn_raw_buffer_store_b64(v2u{__float_as_uint(s.x), (unsigned)c.tag}, c.rws, WS_CS + (col + lane) * 64 + (c.ti & 7) * 8, 0, 16);
    }
    issue<T, 3>(c);
  }
  if constexpr (K == 7) { if (!c.hf) gjob<T>(c); }
  if constexpr (!T && K == 17) {
    const bool on = (r31 >> 4) == h;
    const unsigned ka = on ? L_KVB + (c.hf * 128 + r31) * 16 : L_ZERO, za = on ? L_ZB + c.hf * 128 + h * 16 : L_ZERO;
    f16v num = zero, den = zero;
#pragma unroll
    for (int t = 0; t < 4; ++t) {
      num = mfmaB(ldsr<v4u>(ka + t * 512), c.ph[t], num);
      den = mfmaB(ldsr<v4u>(za + t * 32), c.ph[t], den);
    }
    const float sd = ldsr<float>(L_STD + (32 * c.m + r31) * 4);
    const float r0 = __builtin_amdgcn_rcpf(den[0] + 1e-6f) * sd, r1 = __builtin_amdgcn_rcpf(den[8] + 1e-6f) * sd;
#pragma unroll
    for (int r = 0; r < 16; ++r) num[r] *= r < 8 ? r0 : r1;
    const unsigned ob = WS_A2 + ((2 * c.ti + (c.m >> 1)) * 72 + 9 * c.jj + 7 + c.hf) * 4096 + (2 * (c.m & 1) + (r31 >> 4)) * 1024 + (r15 + 16 * h) * 16;
    __builtin_amdgcn_raw_buffer_store_b128(pk8(num, 0), c.rws, ob, 0, 16);
    __builtin_amdgcn_raw_buffer_store_b128(pk8(num, 8), c.rws, ob + 512, 0, 16);
    SCHED();
  }
  if constexpr (it.kind == WW || it.kind == AA || it.kind == KV) {
    constexpr bool own_issue = K > 12;
    constexpr int pn = pend<T>(K);
#pragma unroll
    for (int i = 0; i < 16; ++i) {
      if constexpr (pn >= 0) c.acc2[(i >> 1) & 3][i & 1] = mfma32(c.b2[2 * pn + (i >> 3)][i & 1], c.af[pn & 1][i >> 1], pn == 0 && i < 8 ? z4f : c.acc2[(i >> 1) & 3][i & 1]);
      if constexpr (it.kind == WW) { if (i < 4) c.b2[2 * it.n + (i >> 1)][i & 1] = trfrag(c.ab2[i & 1] + sl + (i >> 1) * 4096, 2048); }
      if constexpr (it.kind == AA) { if (i < 8) c.af[it.n & 1][i] = ldsr<v4u>(c.aa[i & 3] + sl + (i >> 2) * 4096); }
      if constexpr (own_issue) { if ((i & 3) == 1) issue_rt<T, K + 3>(c, i >> 2); }
      SCHED();
    }
    if constexpr (it.kind == KV) {
      const unsigned q = r15 >> 2, a = sla + w * 4096 + (4 * g + q) * 256 + (lane & 3) * 8;
      f4 s = it.n ? c.kvs : z4f;
#pragma unroll
      for (int b = 0; b < 8; ++b) {
        const unsigned sel = r15 == (unsigned)(8 * it.n + b) ? 0x3f803f80u : 0u;
        s = mfma16(trrd(a + (((b & 3) ^ q) << 5) + (b >> 2) * 128), v2u{sel, sel}, s);
      }
      c.kvs = s;
      if constexpr (it.n == 1) {
        const f4 zp = ldsr<f4>(L_GST + w * 1024 + lane * 16);
        const float z = xg_add((zp.x + zp.y) + (zp.z + zp.w));
        const unsigned L = 4 * r15 + g;
        ldsw<v2u>(L_KVB + (w * 32 + (L & 31)) * 16 + (L >> 5) * 8, pk4(s));
        if (lane < 16) ldsw<uint16_t>(L_ZB + w * 32 + lane * 2, (uint16_t)pk2(z, 0.f));
      }
    }
  }
}

template <int T, int K> DI void windows(St& c) {
  window<T, K>(c);
  if constexpr (K + 1 < nitems<T>()) windows<T, K + 1>(c);
}

template <int T> DI void program(St& c, const uint16_t* wphi, uint16_t* out) {
  constexpr unsigned WSTR = T ? 3584u : 3072u;
  const unsigned lane = c.lane, w = c.w, g = c.g, r15 = c.r15;
  u64* ts = c.ts;
  const unsigned srow = c.ti * 128 + (T * 8 + c.jj) * 8 + w;
  v4u xrow;
  asm volatile("buffer_load_dwordx4 %0, %1, %2, %3 offen" : "=v"(xrow) : "v"(c.vl), "s"(c.rx), "s"(srow * 1024) : "memory");
  c.vx0 = (c.ti * 128 + 16 * w + (lane >> 3)) * 1024u + (((lane & 7) ^ (lane >> 4)) << 4);
  c.vx1 = (c.vx0 ^ 64u) + 8192u;
  c.vw = (8 * w + (lane >> 3)) * WSTR + (T ? ((448u * c.jj) & ~127u) : c.jj * 128u) + (((lane & 7) ^ ((((lane >> 3) & 2) | (w & 1)) << 1)) << 4);
  c.sp1 = T ? (c.jj & 1) * 256 : 1024u;
  c.sp2 = T ? 256 - (c.jj & 1) * 256 : 0u;
  issue<T, 0>(c);
  {
    const unsigned th = c.ti >> 3, so = (c.ti & 7) * 64 * WSTR + (th ? c.sp2 : c.sp1);
    dma<L_GST, 0>(c.m1, c.vw, c.rw, so);
    dma<L_GST + 8192, 0>(c.m1, c.vw, c.rw, so + (T ? 128u : (th ? 0u : 1024u)));
  }
  issue<T, 1>(c);
  TS(11);
  asm volatile("s_waitcnt vmcnt(10)" : "+v"(xrow)::"memory");
  {
    float v[8];
#pragma unroll
    for (int d = 0; d < 4; ++d) { v[2 * d] = blo(xrow[d]); v[2 * d + 1] = bhi(xrow[d]); }
    const float mean = wave_add(((v[0] + v[1]) + (v[2] + v[3])) + ((v[4] + v[5]) + (v[6] + v[7]))) * (1.f / 512.f);
    float q = 0.f;
#pragma unroll
    for (int d = 0; d < 8; ++d) q = __builtin_fmaf(v[d] - mean, v[d] - mean, q);
    const float var = wave_add(q) * (1.f / 512.f);
    const float sd = __builtin_amdgcn_sqrtf(rbf(rbf(var) + 1e-5f));
    if (lane == 0) __builtin_amdgcn_raw_buffer_store_b64(v2u{pk2(mean, sd), (unsigned)c.tag}, c.rws, WS_ST + srow * 8, 0, 16);
  }
  const unsigned q = r15 >> 2;
  if (T) {
    const unsigned f = (q & 2) | (g & 1), rowp = (8 * g + q) * 128 + (lane & 3) * 8;
#pragma unroll
    for (int ks = 0; ks < 2; ++ks) c.ax[ks] = (4 * c.m + (r15 >> 3)) * 1024 + (r15 & 7) * 128 + (((4 * ks + g) ^ (r15 >> 1)) << 4);
#pragma unroll
    for (int t = 0; t < 4; ++t) c.aw[t] = 16384 + c.hf * 8192 + rowp + ((t ^ f) << 5);
    asm volatile("" : "+v"(c.ax[0]), "+v"(c.ax[1]), "+v"(c.aw[0]), "+v"(c.aw[1]), "+v"(c.aw[2]), "+v"(c.aw[3]));
  } else {
    const unsigned r31 = lane & 31, h = lane >> 5;
#pragma unroll
    for (int u = 0; u < 4; ++u) c.ax[u] = (4 * c.m + (r31 >> 3)) * 1024 + (r31 & 7) * 128 + (((2 * u + h) ^ ((r31 >> 1) & 7)) << 4);
    c.aw[0] = (8 * h + q) * 128 + (lane & 3) * 8 + (((2 * c.hf + (g & 1)) ^ ((q & 2) | h)) << 5);
    asm volatile("" : "+v"(c.ax[0]), "+v"(c.ax[1]), "+v"(c.ax[2]), "+v"(c.ax[3]), "+v"(c.aw[0]));
    ldsw<unsigned>(L_ZERO + threadIdx.x * 4, 0u);
  }

  windows<T, 0>(c);

  const unsigned nh = w & 1, kq = w >> 1;
#pragma unroll
  for (int i = 0; i < 16; ++i) c.acc2[(i >> 1) & 3][i & 1] = mfma32(c.b2[16 + (i >> 3)][i & 1], c.af[0][i >> 1], c.acc2[(i >> 1) & 3][i & 1]);
  TS(9);
  if (blockIdx.x == 0 && threadIdx.x == 0) set_flag(c.ws, WS_EPOCH, c.tag);
  constexpr unsigned rb = (((nitems<T>() - 1) & 3) < 2) ? 65536u : 0u;
#pragma unroll
  for (int s = 1; s < 4; ++s)
#pragma unroll
    for (int nf = 0; nf < 2; ++nf) ldsw<f4>(rb + ((((kq ^ s) * 2 + nh) * 3 + (s - 1)) * 2 + nf) * 1024 + lane * 16, c.acc2[s][nf]);
  WAIT_LGKM0();
  BARRIER();
  const float rs = __builtin_amdgcn_rcpf(bhi(c.gst.x));
  v2u o[2];
#pragma unroll
  for (int nf = 0; nf < 2; ++nf) {
    f4 s = c.acc2[0][nf];
#pragma unroll
    for (int k = 0; k < 3; ++k) s += ldsr<f4>(rb + ((w * 3 + k) * 2 + nf) * 1024 + lane * 16);
    o[nf] = pk4(f4{__builtin_fmaf(s.x, rs, blo(c.xr[nf].x)), __builtin_fmaf(s.y, rs, bhi(c.xr[nf].x)), __builtin_fmaf(s.z, rs, blo(c.xr[nf].y)),
                   __builtin_fmaf(s.w, rs, bhi(c.xr[nf].y))});
  }
  auto sx = __builtin_amdgcn_permlane16_swap(o[0].x, o[1].x, false, false);
  auto sy = __builtin_amdgcn_permlane16_swap(o[0].y, o[1].y, false, false);
  const unsigned col = c.c2 * 64 + nh * 32 + (g & 1) * 16 + (g >> 1) * 8;
  *(v4u*)(out + (size_t)(c.r2 * 64 + kq * 16 + r15) * 512 + col) = v4u{sx[0], sy[0], sx[1], sy[1]};
}

__global__ __launch_bounds__(512, 2) void fused_block(const uint16_t* __restrict__ x, const uint16_t* __restrict__ wqkv,
                                                      const uint16_t* __restrict__ wphi, const uint16_t* __restrict__ wo,
                                                      const uint16_t* __restrict__ w1, const uint16_t* __restrict__ w2,
                                                      uint16_t* __restrict__ out, char* __restrict__ ws) {
  __shared__ __attribute__((aligned(1024))) char smem[LDS_BYTES];
  asm volatile("" ::"s"((unsigned)(uintptr_t)(LAS char*)smem));
  u64 ts[16];
#pragma unroll
  for (int i = 0; i < 16; ++i) ts[i] = 0;
  TS(0);
  St c;
  c.ts = ts;
  const unsigned b = blockIdx.x, xcd = b & 7, idx = b >> 3;
  c.lane = threadIdx.x & 63;
  c.w = rfl(threadIdx.x >> 6);
  {
    u64 pc;
    asm volatile("s_getpc_b64 %0" : "=s"(pc));
    if (c.w == 7) {
      unsigned t;
      asm volatile("s_mov_b32 m0, %1\n\ts_mov_b32 %0, 0\n\tbuffer_load_dword %2, %3, %0 offen lds"
                   : "=&s"(t) : "i"(L_KVB), "v"((idx * 16 + (c.lane & 15)) * 128), "s"(mk_rsrc((const void*)(pc & ~127ull), CODE_PF)) : "memory");
    }
  }
  c.m = c.w & 3;
  c.hf = c.w >> 2;
  c.g = c.lane >> 4;
  c.r15 = c.lane & 15;
  c.ti = (xcd & 1) * 8 + (idx >> 2);
  const unsigned type = (idx >> 1) & 1;
  c.jj = (xcd >> 1) * 2 + (idx & 1);
  c.r2 = xcd * 4 + (idx >> 3);
  c.c2 = idx & 7;
  c.m1 = c.w * 1024;
  c.m2 = c.w * 2048;
  c.m4 = c.w * 4096;
  c.sw1 = (224 * c.w + 96 * (c.w & 1)) * 1024;
  c.sw2 = (224 * c.w + 128 - 128 * (c.w & 1)) * 1024;
  c.swo = c.w * 65536;
  c.sa2 = WS_A2 + (c.r2 * 72 + 9 * c.w) * 4096;
  c.skv = WS_KV + (c.jj * 8 + c.w) * 8192;
  c.rx = mk_rsrc(x, 2048u * 1024u);
  c.rw2 = mk_rsrc(w2, 1792u * 1024u);
  c.rwo = mk_rsrc(wo, 512u * 1024u);
  c.rws = mk_rsrc(ws, WS_END);
  c.ws = ws;
  c.vl = c.lane * 16;
  c.vb2 = (c.lane >> 3) * 1024u + c.c2 * 128u + (((c.lane & 7) ^ (((c.lane >> 4) & 3) << 1)) << 4);
  c.tag = *(const __attribute__((address_space(4))) u64*)(uintptr_t)(ws + WS_EPOCH) + 1;
  if (type) {
    c.rw = mk_rsrc(w1, 512u * 3584u);
    program<1>(c, wphi, out);
  } else {
    c.rw = mk_rsrc(wqkv, 512u * 3072u);
    if (c.w < 2) dma<L_WPHI, 0>(c.m1, c.vl, mk_rsrc(wphi, 32u * 512u), c.jj * 2048 + c.m1);
    program<0>(c, wphi, out);
  }
}

extern "C" void kernel_launch(void* const* d_in, const int* in_sizes, int n_in, void* d_out, int out_size, void* d_ws,
                              size_t ws_size, hipStream_t stream) {
  fused_block<<<256, 512, 0, stream>>>((const uint16_t*)d_in[0], (const uint16_t*)d_in[1], (const uint16_t*)d_in[2],
                                       (const uint16_t*)d_in[3], (const uint16_t*)d_in[5], (const uint16_t*)d_in[6],
                                       (uint16_t*)d_out, (char*)d_ws);
}
```

```cpp
#include <hip/hip_runtime.h>
#include <stdint.h>

typedef unsigned v4u __attribute__((ext_vector_type(4)));
typedef unsigned v2u __attribute__((ext_vector_type(2)));
typedef float f4 __attribute__((ext_vector_type(4)));
typedef float f2 __attribute__((ext_vector_type(2)));
typedef float f8v __attribute__((ext_vector_type(8)));
typedef float f16v __attribute__((ext_vector_type(16)));
typedef short s8 __attribute__((ext_vector_type(8)));
typedef short s4 __attribute__((ext_vector_type(4)));
typedef short s2 __attribute__((ext_vector_type(2)));
typedef __bf16 bf2 __attribute__((ext_vector_type(2)));
typedef unsigned long long u64;
#define GAS __attribute__((address_space(1)))
#define LAS __attribute__((address_space(3)))
#define DI __device__ __forceinline__
#define RLX_AGENT __ATOMIC_RELAXED, __HIP_MEMORY_SCOPE_AGENT

#define PROFILE 0
#define MIX 0
#define ROT2 0
#define ROTW 1
#define W_PF 1
#define CODE_PF 40960

constexpr unsigned WS_EPOCH = 0;
constexpr unsigned WS_FKV = 32768;
constexpr unsigned WS_FA = 40960;
constexpr unsigned WS_ST = 16384;
constexpr unsigned WS_Z = 65536;
constexpr unsigned WS_CS = 131072;
constexpr unsigned WS_KV = 393216;
constexpr unsigned WS_A2 = WS_KV + (1u << 19);
constexpr unsigned WS_END = WS_A2 + 32u * 72u * 4096u;
constexpr unsigned WS_TS = 16u << 20;

constexpr unsigned L_GST = 131072;
constexpr unsigned L_MISC = 147456;
constexpr unsigned L_CSB = L_MISC;
constexpr unsigned L_STM = L_MISC + 2048;
constexpr unsigned L_STR = L_MISC + 3072;
constexpr unsigned L_STD = L_MISC + 3584;
constexpr unsigned L_WPHI = L_MISC + 4096;
constexpr unsigned L_ZRED = L_MISC + 6144;
constexpr unsigned L_ZERO = L_MISC + 8192;
constexpr unsigned L_KVB = L_MISC + 10240;
constexpr unsigned L_ZB = L_MISC + 14336;
constexpr unsigned L_SDB = L_MISC + 14592;
constexpr unsigned LDS_BYTES = 163840;

typedef __amdgpu_buffer_rsrc_t rsrc_t;
DI rsrc_t mk_rsrc(const void* p, unsigned bytes) { return __builtin_amdgcn_make_buffer_rsrc((void*)p, (short)0, (int)bytes, 0x00020000); }

template <unsigned MC, unsigned SC, bool SC1 = false> DI void dma(unsigned mb, unsigned voff, rsrc_t rs, unsigned sb) {
  unsigned t;
  if constexpr (SC1)
    asm volatile("s_add_u32 m0, %1, %2\n\ts_add_u32 %0, %3, %4\n\tbuffer_load_dwordx4 %5, %6, %0 offen sc1 lds"
                 : "=&s"(t) : "s"(mb), "i"(MC), "s"(sb), "i"(SC), "v"(voff), "s"(rs) : "memory", "scc");
  else
    asm volatile("s_add_u32 m0, %1, %2\n\ts_add_u32 %0, %3, %4\n\tbuffer_load_dwordx4 %5, %6, %0 offen lds"
                 : "=&s"(t) : "s"(mb), "i"(MC), "s"(sb), "i"(SC), "v"(voff), "s"(rs) : "memory", "scc");
}
DI void touch(rsrc_t rs, unsigned voff) {
  unsigned t;
  asm volatile("s_mov_b32 m0, %1\n\ts_mov_b32 %0, 0\n\tbuffer_load_dword %2, %3, %0 offen lds" : "=&s"(t) : "i"(L_KVB), "v"(voff), "s"(rs) : "memory");
}
#define WAIT_VM(n) asm volatile("s_waitcnt vmcnt(%0)" ::"i"(n) : "memory")
#define WAIT_LGKM0() do { __builtin_amdgcn_s_waitcnt(0xC07F); asm volatile("" ::: "memory"); } while (0)
#define BARRIER() asm volatile("s_barrier" ::: "memory")
#define SCHED() __builtin_amdgcn_sched_barrier(0)
#define WL(v_, l_) asm volatile("v_writelane_b32 %0, %1, %2" : "+v"(c.st) : "s"((unsigned)(v_)), "i"(l_))
#define PIN(x) asm volatile("" : "+v"(x))

DI f4 mfma32(v4u a, v4u b, f4 c) { return __builtin_amdgcn_mfma_f32_16x16x32_bf16(__builtin_bit_cast(s8, a), __builtin_bit_cast(s8, b), c, 0, 0, 0); }
DI f4 mfma16(v2u a, v2u b, f4 c) { return __builtin_amdgcn_mfma_f32_16x16x16bf16_1k(__builtin_bit_cast(s4, a), __builtin_bit_cast(s4, b), c, 0, 0, 0); }
DI f16v mfmaB(v4u a, v4u b, f16v c) { return __builtin_amdgcn_mfma_f32_32x32x16_bf16(__builtin_bit_cast(s8, a), __builtin_bit_cast(s8, b), c, 0, 0, 0); }
DI f16v mfmaB8(v2u a, v2u b, f16v c) { return __builtin_amdgcn_mfma_f32_32x32x8bf16_1k(__builtin_bit_cast(s4, a), __builtin_bit_cast(s4, b), c, 0, 0, 0); }
DI unsigned pk2(float a, float b) { bf2 r = {(__bf16)a, (__bf16)b}; return __builtin_bit_cast(unsigned, r); }
DI v2u pk4(f4 v) { return v2u{pk2(v.x, v.y), pk2(v.z, v.w)}; }
DI v4u pk8(const f16v& v, int o) { return v4u{pk2(v[o], v[o + 1]), pk2(v[o + 2], v[o + 3]), pk2(v[o + 4], v[o + 5]), pk2(v[o + 6], v[o + 7])}; }
DI unsigned relu2(unsigned p) { return __builtin_bit_cast(unsigned, __builtin_elementwise_max(__builtin_bit_cast(s2, p), s2{0, 0})); }
DI float rbf(float a) { return __uint_as_float(pk2(a, 0.f) << 16); }
DI float blo(unsigned p) { return __uint_as_float(p << 16); }
DI float bhi(unsigned p) { return __uint_as_float(p & 0xffff0000u); }
DI v2u trrd(unsigned addr) { return __builtin_bit_cast(v2u, __builtin_amdgcn_ds_read_tr16_b64_v4i16((LAS s4*)(uintptr_t)addr)); }
DI v4u cat(v2u a, v2u b) { return v4u{a.x, a.y, b.x, b.y}; }
DI v4u trfrag(unsigned addr, unsigned off2) { return cat(trrd(addr), trrd(addr + off2)); }
template <class T> DI T ldsr(unsigned addr) { return *(LAS T*)(uintptr_t)addr; }
template <class T> DI void ldsw(unsigned addr, T v) { *(LAS T*)(uintptr_t)addr = v; }
DI unsigned rfl(unsigned x) { return __builtin_amdgcn_readfirstlane(x); }
DI float max3(float a, float b, float c) { return __builtin_elementwise_maximum(__builtin_elementwise_maximum(a, b), c); }
DI float min3(float a, float b, float c) { return __builtin_elementwise_minimum(__builtin_elementwise_minimum(a, b), c); }
template <int CTRL> DI float dpp_add(float x) { return x + __builtin_bit_cast(float, __builtin_amdgcn_update_dpp(0, __builtin_bit_cast(int, x), CTRL, 0xf, 0xf, false)); }

DI float xg_add(float x) {
  auto a = __builtin_amdgcn_permlane16_swap(__float_as_uint(x), __float_as_uint(x), false, false);
  x = __uint_as_float(a[0]) + __uint_as_float(a[1]);
  auto b = __builtin_amdgcn_permlane32_swap(__float_as_uint(x), __float_as_uint(x), false, false);
  return __uint_as_float(b[0]) + __uint_as_float(b[1]);
}
DI float wave_add(float x) { return xg_add(dpp_add<0x121>(dpp_add<0x122>(dpp_add<0x124>(dpp_add<0x128>(x))))); }
DI float sum16(const f16v& v) {
  const f8v a = __builtin_shufflevector(v, v, 0, 1, 2, 3, 4, 5, 6, 7) + __builtin_shufflevector(v, v, 8, 9, 10, 11, 12, 13, 14, 15);
  const f4 b = __builtin_shufflevector(a, a, 0, 1, 2, 3) + __builtin_shufflevector(a, a, 4, 5, 6, 7);
  const f2 c = __builtin_shufflevector(b, b, 0, 1) + __builtin_shufflevector(b, b, 2, 3);
  return c.x + c.y;
}
DI float max16(const f16v& p) {
  return max3(max3(max3(p[0], p[1], p[2]), max3(p[3], p[4], p[5]), max3(p[6], p[7], p[8])),
              max3(max3(p[9], p[10], p[11]), max3(p[12], p[13], p[14]), p[15]), p[15]);
}
DI float min16(const f16v& p) {
  return min3(min3(min3(p[0], p[1], p[2]), min3(p[3], p[4], p[5]), min3(p[6], p[7], p[8])),
              min3(min3(p[9], p[10], p[11]), min3(p[12], p[13], p[14]), p[15]), p[15]);
}
DI void softmax_exp(const f16v& p, float sc, float a, float b, f16v& e, f16v& n, int r0) {
#pragma unroll
  for (int r = r0; r < r0 + 4; r += 2) {
    const f2 x = {p[r], p[r + 1]};
    const f2 u = __builtin_elementwise_fma(x, f2{sc, sc}, f2{a, a}), v = __builtin_elementwise_fma(x, f2{-sc, -sc}, f2{b, b});
    e[r] = __builtin_amdgcn_exp2f(u.x);
    e[r + 1] = __builtin_amdgcn_exp2f(u.y);
    n[r] = __builtin_amdgcn_exp2f(v.x);
    n[r + 1] = __builtin_amdgcn_exp2f(v.y);
  }
}
DI void softmax_norm(f16v e, float os, v4u& o0, v4u& o1) {
  e *= __builtin_amdgcn_rcpf(sum16(e)) * os;
  o0 = pk8(e, 0);
  o1 = pk8(e, 8);
}

constexpr unsigned SPIN_CAP = 1u << 18;
DI void set_flag(char* ws, unsigned off, u64 tag) { __hip_atomic_store((GAS u64*)(ws + off), tag, RLX_AGENT); }

#define TS(k)

enum Kind { P1, P2, WW, AA, KV, RED, END };
struct Item { int kind, n, ld = 3; };
#define SEQ_B {P2,0},{WW,0},{P2,1},{WW,1},{P2,2},{WW,2},{P2,3},{WW,3},{WW,4},{AA,0},{WW,5},{AA,1},{WW,6},{AA,2},{WW,7},{AA,3},{WW,8},{AA,4},{AA,5},{AA,6},{AA,7,2},{AA,8}
#define SEQ_A {WW,3},{WW,4},{WW,5},{AA,0},{WW,6},{KV,0,2},{KV,1},{AA,1},{WW,7},{AA,2},{WW,8},{AA,3},{AA,4},{AA,5},{AA,6},{AA,7,2},{AA,8}
constexpr Item seqB[] = {{P1,0},{P1,1},{P1,2},{P1,3},{P1,4},{P1,5},{P1,6},{P1,7}, SEQ_B};
constexpr Item seqA[] = {{P1,0},{P1,1},{P2,0},{P1,2},{P1,3},{P1,4},{P2,1},{P1,5},{P1,6},{P1,7},{WW,0},{WW,1},{WW,2},{RED,0}, SEQ_A};
template <int T> constexpr int nitems() { return T ? (int)(sizeof(seqB) / sizeof(Item)) : (int)(sizeof(seqA) / sizeof(Item)); }
template <int T> constexpr Item item_at(int k) { return k < 0 || k >= nitems<T>() ? Item{END, 0} : (T ? seqB[k] : seqA[k]); }
template <int T> constexpr int nops(int k) { return item_at<T>(k).kind == END || item_at<T>(k).kind == RED ? 0 : 4; }
template <int T> constexpr int find(int kind, int n) {
  for (int k = 0; k < nitems<T>(); ++k)
    if (item_at<T>(k).kind == kind && item_at<T>(k).n == n) return k;
  return -100;
}
template <int T> constexpr int iw(int k) {
  int w = -1;
  for (int j = 2; j <= k; ++j) {
    const int x = j - item_at<T>(j).ld;
    w = x > w ? x : (w < 0 ? 0 : w);
  }
  return w;
}
template <int T> constexpr int first_req(int W) {
  for (int k = 0; k < nitems<T>(); ++k)
    if (iw<T>(k) == W && nops<T>(k)) return k;
  return 1000;
}
template <int T> constexpr int nreq(int W) {
  int n = 0;
  for (int k = 0; k < nitems<T>(); ++k)
    if (iw<T>(k) == W) n += nops<T>(k);
  return n;
}
template <int T> constexpr int gate_of(int k) {
  const Item it = item_at<T>(k);
  if (it.kind == KV && it.n == 0) return 3;
  return it.kind == AA ? (it.n == 0 ? 0 : (it.n == 4 ? 1 : (it.n == 7 ? 2 : -1))) : -1;
}
#define LEAD_KV 1
#define LEAD_AA 2
constexpr int lead(int gt) { return gt == 3 ? LEAD_KV : LEAD_AA; }
template <int T> constexpr int gate_in(int W) {
  for (int k = 0; k < nitems<T>(); ++k)
    if (iw<T>(k) == W && gate_of<T>(k) >= 0) return gate_of<T>(k);
  return -1;
}
template <int T> constexpr bool lagged(int k) { return item_at<T>(k).kind == AA || (T && item_at<T>(k).kind == P2); }
template <int T> constexpr bool split(int k) { return lagged<T>(k) && !lagged<T>(k + 1) && item_at<T>(k + 2).kind != END; }
struct Due { int k, part; };
template <int T> constexpr Due due(int K) {
  if (lagged<T>(K - 1)) return Due{K - 1, split<T>(K - 1) ? 1 : 0};
  if (split<T>(K - 2)) return Due{K - 2, 2};
  return Due{-1, 0};
}
constexpr int KB_E2 = find<1>(P2, 3) + (split<1>(find<1>(P2, 3)) ? 2 : 1);
constexpr int KA_K = find<0>(P1, 7) + 1, KA_RED = find<0>(RED, 0), KA_S = KA_RED + 1, KA_O = find<0>(KV, 1);
static_assert(KA_RED == KA_K + 3, "");
template <int T> constexpr int kend() { return find<T>(P1, 7); }
#define QCAP 12
constexpr int qdone(int K) {
  int d = 0;
  for (int k = 1; k < K && k < KA_K; ++k) {
    int room = QCAP - (item_at<0>(k - 1).kind == P1 ? 8 : 0);
    while (room > 0 && d < 32 && find<0>(P1, d >> 2) < k && find<0>(P2, d >> 4) < k) { ++d; --room; }
  }
  return d;
}
struct Ev { int win, n, after, fw; };
#define FW_H1 3
#define FW_H2 3
#define FW_KV 2
#define FW_O 3
template <int T> constexpr Ev ev(int i) {
  return T ? (i ? Ev{KB_E2, 4, 1, KB_E2 + FW_H2} : Ev{8, 4, 1, 8 + FW_H1}) : (i ? Ev{KA_O, 2, 0, KA_O + FW_O} : Ev{KA_RED, 2, 0, KA_RED + FW_KV});
}
template <int T> constexpr int allowed(int K) {
  if (K == 0) return 5;
  const int need = item_at<T>(K).kind == KV && item_at<T>(K).n == 0 ? K + 1 : K;
  int n = K == 1 ? 1 : 0;
  for (int k = need + 1; k < nitems<T>(); ++k)
    if (iw<T>(k) < K) n += nops<T>(k);
  for (int i = 0; i < 2; ++i) {
    const Ev e = ev<T>(i);
    if (e.win < K && e.win + e.after > iw<T>(need)) n += e.n;
  }
  if (!nops<T>(need)) n = 63;
  for (int i = 0; i < 2; ++i) {
    const Ev e = ev<T>(i);
    if (K == e.fw) {
      int y = 0;
      for (int k = e.win + e.after; k < K; ++k) y += nreq<T>(k);
      if (y < n) n = y;
    }
  }
  return n;
}

struct St {
  rsrc_t rx, rw, rw2, rwo, rws, rout;
  char* ws;
  u64 tag;
  unsigned lane, w, m, hf, g, r15, ti, jj, r2, c2, rot, rk;
  unsigned m1, m2, m4;
  unsigned sp1, sp2, sw1, sw2, swo, sa2, skv;
  unsigned vx0, vx1, vw, vb2, vl;
  unsigned ax[4], aw[4], aw2[3], ab2[2], aa[4];
  v4u X[8][4];
  v2u wb[2][12][2], wq[32][2];
  f4 acc[4][2], acc1[3][2];
  f16v kk, vv, qq;
  v4u wp0, wp1, ph[4];
  f16v se, sn;
  v4u vb0, vb1;
  float sa, sb, sc;
  v4u b2[18][2], af[2][8];
  f4 acc2[4][2];
  v2u gate[4], xr[2], gst;
  u64* ts;
  u64 t0, t1;
  unsigned st;
};

template <int n> DI unsigned phys(const St& c) {
  if (n < 4) return (n + c.rot) & 3;
  if (n >= 7) return (n - 7 + c.rot) & 1;
  const unsigned t = n - 4 + c.rot;
  return t >= 3 ? t - 3 : t;
}
template <int T, int K, int p> DI void issue_piece(St& c) {
  constexpr Item it = item_at<T>(K);
  constexpr unsigned sl = (K & 3) * 32768u, WSTR = T ? 3584u : 3072u, n = it.n;
  if constexpr (it.kind == P1) {
    const unsigned kc = (n + c.rk) & 7;
    if constexpr (p < 2) dma<sl + p * 1024, 0>(c.m2, p ? c.vx1 : c.vx0, c.rx, kc * 128);
    else dma<sl + 16384 + (p - 2) * 8192, (p - 2) * (T ? 128 : 1024)>(c.m1, c.vw, c.rw, c.sp1 + kc * 64 * WSTR);
  } else if constexpr (it.kind == P2) {
    dma<sl + p * 8192, (T ? (p & 1) * 128 : 0)>(c.m1, c.vw, c.rw, c.sp2 + (((T ? 2 * n + (p >> 1) : 4 * n + p) + c.rk) & 7) * 64 * WSTR);
  } else if constexpr (it.kind == WW) {
    if constexpr (n < 4) dma<sl + p * 1024, 8 * p * 1024>(c.m4, c.vb2, c.rw2, c.sw1 + phys<n>(c) * 32768);
    else if constexpr (n < 7) dma<sl + p * 1024, 8 * p * 1024>(c.m4, c.vb2, c.rw2, c.sw2 + phys<n>(c) * 32768);
    else dma<sl + p * 1024, (16 * (p & 1) + 8 * (p >> 1)) * 1024>(c.m4, c.vb2, c.rwo, c.swo + phys<n>(c) * 32768);
  } else if constexpr (it.kind == AA) {
    dma<sl + p * 1024, (n < 4 ? 0 : (n < 7 ? 4 : 7)) * 4096 + p * 1024, true>(c.m4, c.vl, c.rws, c.sa2 + phys<n>(c) * 4096);
  } else if constexpr (it.kind == KV) {
    dma<sl + p * 1024, n * 256 + p * 2048, true>(c.m4, c.g * 512 + ((c.r15 ^ (c.g << 1)) << 4), c.rws, c.skv);
  }
}
template <int T, int K> DI void issue_rt(St& c, int p) {
  if (p == 0) issue_piece<T, K, 0>(c);
  if (p == 1) issue_piece<T, K, 1>(c);
  if (p == 2) issue_piece<T, K, 2>(c);
  if (p == 3) issue_piece<T, K, 3>(c);
}
template <int T, int W> DI void request(St& c, int q) {
  constexpr int k = first_req<T>(W), n = nreq<T>(W);
  static_assert(n == 0 || n == 4 || (n == 8 && nops<T>(k + 1) == 4 && iw<T>(k + 1) == W), "");
  if constexpr (n == 4) issue_rt<T, k>(c, q);
  if constexpr (n == 8) {
    if (q < 2) { issue_rt<T, k>(c, 2 * q); issue_rt<T, k>(c, 2 * q + 1); }
    else { issue_rt<T, k + 1>(c, 2 * q - 4); issue_rt<T, k + 1>(c, 2 * q - 3); }
  }
}
template <int T, int K> DI void issue(St& c) {
  issue_piece<T, K, 0>(c);
  issue_piece<T, K, 1>(c);
  issue_piece<T, K, 2>(c);
  issue_piece<T, K, 3>(c);
}
DI void snapshot(St& c, v2u& r, unsigned addr) { asm volatile("buffer_load_dwordx2 %0, %1, %2, 0 offen sc1" : "=v"(r) : "v"(addr), "s"(c.rws) : "memory"); }
template <int N> DI void gate(St& c, v2u& r, unsigned addr) {
  asm volatile("s_waitcnt vmcnt(%1)" : "+v"(r) : "i"(N) : "memory");
  if (!__all(r.x == (unsigned)c.tag && r.y == (unsigned)(c.tag >> 32))) {
    GAS u64* p = (GAS u64*)(c.ws + addr);
#pragma unroll 1
    for (unsigned spins = 0; spins < SPIN_CAP; ++spins) {
      if (__all(__hip_atomic_load(p, RLX_AGENT) == c.tag)) break;
      __builtin_amdgcn_s_sleep(2);
    }
  }
  asm volatile("" ::: "memory");
}
DI unsigned gate_addr(const St& c, int gt) {
  return gt == 3 ? WS_FKV + (c.jj * 8 + c.w) * 128 + c.r15 * 8
                 : WS_FA + (((c.r2 >> 1) * 3 + gt) * 8 + c.w) * 64 + (2 * (c.r2 & 1) + (c.lane & 1) + 2 * (c.lane & 2)) * 8;
}
DI unsigned misc(unsigned lanepart) {
  unsigned a = L_MISC + lanepart;
  PIN(a);
  return a;
}
template <int T> DI void gload(St& c) {
  const unsigned col = T ? 1536 + 224 * c.jj + 32 * c.w : (c.w < 6 ? c.w >> 1 : 2u) * 512 + 64 * c.jj + 32 * (c.w & 1);
  dma<L_GST, WS_CS, true>(c.m2, c.vl, c.rws, col * 64);
  dma<L_GST + 1024, WS_CS + 1024, true>(c.m2, c.vl, c.rws, col * 64);
  if (c.w < 2) dma<L_ZRED, WS_ST, true>(c.m1, c.vl, c.rws, c.ti * 1024);
}
template <int T> DI void gjob(St& c) {
  const unsigned lane = c.lane, w = c.w, tg = (unsigned)c.tag;
  v4u g0, g1;
  v2u gs;
#pragma unroll 1
  for (unsigned spins = 0; spins < SPIN_CAP; ++spins) {
    g0 = ldsr<v4u>(L_GST + threadIdx.x * 32);
    g1 = ldsr<v4u>(L_GST + threadIdx.x * 32 + 16);
    gs = ldsr<v2u>(L_ZRED + (w & 1) * 1024 + (threadIdx.x & 127) * 8);
    const bool ok = ((g0.y == tg && g0.w == tg && g1.y == tg && g1.w == tg) || (T && w == 7)) && (gs.y == tg || w >= 2);
    if (__all(ok)) break;
    gload<T>(c);
    WAIT_VM(0);
  }
  const float s = dpp_add<0xB1>((__uint_as_float(g0.x) + __uint_as_float(g0.z)) + (__uint_as_float(g1.x) + __uint_as_float(g1.z)));
  const float hi = rbf(s), mid = rbf(s - hi);
  if (!(lane & 1)) ldsw<v2u>(L_CSB + (threadIdx.x >> 1) * 8, v2u{pk2(hi, mid), pk2((s - hi) - mid, 0.f)});
  if (w < 2) {
    const unsigned nm = (gs.x & 0xffffu) ^ 0x8000u;
    ldsw<v2u>(L_STM + threadIdx.x * 8, v2u{nm | (nm << 16), nm});
    if (!T) {
      ldsw<float>(L_STR + threadIdx.x * 4, __builtin_amdgcn_rcpf(bhi(gs.x)));
      ldsw<float>(L_STD + threadIdx.x * 4, bhi(gs.x));
      ldsw<uint16_t>(L_SDB + threadIdx.x * 2, (uint16_t)(gs.x >> 16));
    }
  }
  SCHED();
}

template <int T, int K> DI void window(St& c) {
  constexpr Item it = item_at<T>(K);
  constexpr unsigned sl = (K & 1) * 32768u;
  constexpr unsigned hi = (K & 2) * 32768u, sla = sl + hi;
  constexpr int KW = find<T>(WW, 0), KA = find<T>(AA, 0);
  const unsigned lane = c.lane, w = c.w, g = c.g, r15 = c.r15, r31 = lane & 31, h = lane >> 5;
  u64* ts = c.ts;
  WAIT_LGKM0();
  constexpr int alw = allowed<T>(K);
  WAIT_VM(alw);
  if constexpr (K == ev<T>(0).fw) { if (lane == 0) set_flag(c.ws, T ? WS_FA + (c.ti * 24 + c.jj) * 64 + w * 8 : WS_FKV + (c.jj * 8 + w) * 128 + c.ti * 8, c.tag); TS(3); }
  if constexpr (K == ev<T>(1).fw) { if (lane == 0) set_flag(c.ws, WS_FA + (c.ti * 24 + (T ? 8 : 16) + c.jj) * 64 + w * 8, c.tag); TS(T ? 4 : 6); }
  BARRIER();
  SCHED();
  if constexpr (K == 0) TS(12);
  if constexpr (K == kend<T>() + 1) TS(1);
  if constexpr (K == (T ? KB_E2 : KA_RED)) TS(2);
  if constexpr (it.kind == AA && it.n == 0) TS(7);
  if constexpr (it.kind == AA && it.n == 4) TS(8);
  if constexpr (it.kind == AA && it.n == 7) TS(13);
  if constexpr (it.kind == KV && it.n == 0) TS(5);
  if constexpr (gate_in<T>(K) >= 0) {
    constexpr int gt = gate_in<T>(K);
    gate<nreq<T>(K - 1) + (lead(gt) > 1 ? nreq<T>(K - 2) : 0)>(c, c.gate[gt], gate_addr(c, gt));
    if constexpr (gt == 0 && T) TS(5);
    if constexpr (gt == 1 && T) TS(6);
    if constexpr (gt == 2) TS(14);
    if constexpr (gt == 3) {
      TS(4);
      dma<L_GST, WS_Z, true>(c.m1, r15 * 64 + g * 16, c.rws, (c.jj * 8 + w) * 1024);
    }
  }
  constexpr int g1 = gate_in<T>(K + 1), g2 = gate_in<T>(K + 2);
  if constexpr (g1 >= 0 && lead(g1) == 1) snapshot(c, c.gate[g1], gate_addr(c, g1));
  if constexpr (g2 >= 0 && lead(g2) == 2) snapshot(c, c.gate[g2], gate_addr(c, g2));
  if constexpr (K == 1) {
    if (w == 6) touch(c.rw2, blockIdx.x * 7168 + (lane < 56 ? lane : 0) * 128);
    if (w == 5) touch(c.rwo, blockIdx.x * 2048 + r15 * 128);
    if (w == 4) touch(c.rout, (c.r2 * 64 + lane) * 1024 + c.c2 * 128);
  }
  if constexpr (K == kend<T>() - 2) gload<T>(c);
  if constexpr (K == nitems<T>() - 5) {
    const unsigned row = c.r2 * 64 + (w >> 1) * 16 + r15;
    const unsigned xo = row * 1024 + (c.c2 * 64 + (w & 1) * 32 + 4 * g) * 2;
    c.xr[0] = __builtin_amdgcn_raw_buffer_load_b64(c.rx, xo, 0, 0);
    c.xr[1] = __builtin_amdgcn_raw_buffer_load_b64(c.rx, xo + 32, 0, 0);
    c.gst = __builtin_amdgcn_raw_buffer_load_b64(c.rws, WS_ST + row * 8, 0, 16);
    asm volatile("" ::: "memory");
  }
  if constexpr (K > 0 && !(K & 1)) {
    if constexpr (K <= kend<T>()) {
#pragma unroll
      for (int t = 0; t < (T ? 2 : 4); ++t) { c.ax[t] ^= 65536u; PIN(c.ax[t]); }

#pragma unroll
      for (int t = 0; t < (T ? 4 : 1); ++t) { c.aw[t] ^= 65536u; PIN(c.aw[t]); }
    }
    if constexpr (T && K > 8 && K <= find<1>(P2, 3)) {
#pragma unroll
      for (int t = 0; t < 3; ++t) { c.aw2[t] ^= 65536u; PIN(c.aw2[t]); }
    }
    if constexpr (K > KW && K <= find<T>(WW, 8)) {
#pragma unroll
      for (int t = 0; t < 2; ++t) { c.ab2[t] ^= 65536u; PIN(c.ab2[t]); }
    }
    if constexpr (K > KA) {
#pragma unroll
      for (int t = 0; t < 4; ++t) { c.aa[t] ^= 65536u; PIN(c.aa[t]); }
    }
  }
  if constexpr (T && K == 8) {
    const unsigned q = r15 >> 2;
#pragma unroll
    for (int t = 0; t < 3; ++t) {
      const unsigned lu = (c.jj & 1) * 2 + (t < 2 ? 4 * c.hf + t : 2 + c.hf);
      c.aw2[t] = (lu >> 2) * 8192 + (8 * g + q) * 128 + (lane & 3) * 8 + (((lu & 3) ^ ((q & 2) | (g & 1))) << 5);
    }
  }
  if constexpr (K == KW) {
    const unsigned q = r15 >> 2;
#pragma unroll
    for (int nf = 0; nf < 2; ++nf) { c.ab2[nf] = hi + (w >> 1) * 8192 + (4 * g + q) * 128 + (((2 * (w & 1) + nf) ^ ((q >> 1) | ((g & 1) << 1))) << 5) + (lane & 3) * 8; PIN(c.ab2[nf]); }
  }
  if constexpr (K == KA) {
#pragma unroll
    for (int s = 0; s < 4; ++s) { c.aa[s] = hi + (w >> 1) * 8192 + ((s ^ (w >> 1)) << 10) + lane * 16; PIN(c.aa[s]); }
  }
  const f4 z4f = {0.f, 0.f, 0.f, 0.f};
  f16v zero;
#pragma unroll
  for (int r = 0; r < 16; ++r) zero[r] = 0.f;

  if constexpr (K == kend<T>()) { if (c.hf) gjob<T>(c); }
  if constexpr (!T && K == KA_RED) {
    const unsigned ra = sla + (w & 4) * 4096 + (w & 3) * 1024 + lane * 16;
    const unsigned za = misc((w >> 1) * 512 + (16 * (r15 >> 3) + 8 * (w & 1) + (r15 & 7)) * 4) + (L_ZRED - L_MISC);
    f4 s = ldsr<f4>(ra);
    float z = ldsr<float>(za);
#pragma unroll
    for (int k = 1; k < 4; ++k) {
      s += ldsr<f4>(ra + k * 4096);
      z += ldsr<float>(za + k * 128);
    }
    __builtin_amdgcn_raw_buffer_store_b64(pk4(s), c.rws, lane * 8 + WS_KV + ((c.jj * 8 + w) * 16 + c.ti) * 512, 0, 16);
    if (lane < 16) __builtin_amdgcn_raw_buffer_store_b32(__float_as_uint(z), c.rws, WS_Z + (c.jj * 8 + w) * 1024 + lane * 64 + c.ti * 4, 0, 16);
    SCHED();
  }
  if constexpr (it.kind == KV && it.n == 0) {
    const unsigned q = r15 >> 2, a = w * 4096 + (4 * g + q) * 256 + (lane & 3) * 8;
    f4 s[2] = {z4f, z4f};
#pragma unroll
    for (int b = 0; b < 16; ++b) {
      const unsigned sel = r15 == (unsigned)b ? 0x3f803f80u : 0u;
      s[b & 1] = mfma16(trrd(((K + (b >> 3)) & 3) * 32768u + a + (((b & 3) ^ q) << 5) + ((b >> 2) & 1) * 128), v2u{sel, sel}, s[b & 1]);
    }
    const f4 zp = ldsr<f4>(L_GST + w * 1024 + lane * 16);
    const float z = xg_add((zp.x + zp.y) + (zp.z + zp.w));
    const unsigned L = 4 * r15 + g;
    ldsw<v2u>(L_KVB + (w * 32 + (L & 31)) * 16 + (L >> 5) * 8, pk4(s[0] + s[1]));
    if (lane < 16) ldsw<uint16_t>(L_ZB + w * 32 + lane * 2, (uint16_t)pk2(z, 0.f));
    SCHED();
  }
  if constexpr (!T && K == KA_O) {
    const bool on = (r31 >> 4) == h;
    const unsigned ka = on ? L_KVB + (c.hf * 128 + r31) * 16 : L_ZERO, za = on ? L_ZB + c.hf * 128 + h * 16 : L_ZERO;
    f16v num = zero, den = zero;
#pragma unroll
    for (int t = 0; t < 4; ++t) {
      num = mfmaB(ldsr<v4u>(ka + t * 512), c.ph[t], num);
      den = mfmaB(ldsr<v4u>(za + t * 32), c.ph[t], den);
    }
    const float sd = ldsr<float>(L_STD + (32 * c.m + r31) * 4);
    const float r0 = __builtin_amdgcn_rcpf(den[0] + 1e-6f) * sd, r1 = __builtin_amdgcn_rcpf(den[8] + 1e-6f) * sd;
#pragma unroll
    for (int r = 0; r < 16; ++r) num[r] *= r < 8 ? r0 : r1;
    const unsigned ob = WS_A2 + ((2 * c.ti + (c.m >> 1)) * 72 + 9 * c.jj + 7 + c.hf) * 4096 + (2 * (c.m & 1) + (r31 >> 4)) * 1024 + (r15 + 16 * h) * 16;
    __builtin_amdgcn_raw_buffer_store_b128(pk8(num, 0), c.rws, ob, 0, 16);
    __builtin_amdgcn_raw_buffer_store_b128(pk8(num, 8), c.rws, ob + 512, 0, 16);
    SCHED();
  }
  constexpr bool EP = T && (K == 8 || K == KB_E2);
  constexpr int NUN = K == 8 ? 4 : 3;
  v2u cf[4], nm[2];
  auto ep_reads = [&]() {
  if constexpr (EP) {
    const unsigned ca = misc((((K == 8 ? (c.jj & 1) * 6 : 8 - (c.jj & 1) * 8) + 4 * c.hf) * 16 + r15) * 8), na = misc((32 * c.m + r15) * 8);
#pragma unroll
    for (int t = 0; t < NUN; ++t) cf[t] = ldsr<v2u>(ca + (L_CSB - L_MISC) + t * 128 - (K != 8 && t == 2 ? c.hf * 384 : 0));
#pragma unroll
    for (int mi = 0; mi < 2; ++mi) nm[mi] = ldsr<v2u>(na + (L_STM - L_MISC) + mi * 128);
  }
  };
  auto mmA1 = [&](int kc, int i) {
    const v4u wf = cat(c.wb[kc & 1][i][0], c.wb[kc & 1][i][1]), xf = c.X[kc][i >> 1];
    if (i & 1) c.vv = mfmaB(xf, wf, kc == 0 && i < 2 ? zero : c.vv);
    else c.kk = mfmaB(wf, xf, kc == 0 && i < 2 ? zero : c.kk);
  };
  auto mmQ = [&](int j) { c.qq = mfmaB(cat(c.wq[j][0], c.wq[j][1]), c.X[j >> 2][j & 3], j == 0 ? zero : c.qq); };
  v2u jn;
  unsigned ja, jt;
  v4u tf[2][2], sdf[2];
  auto job = [&](int part) {
    if constexpr (!T && (K == KA_K || K == KA_S)) {
      constexpr bool isk = K == KA_K;
      if (part == 0) {
        const unsigned a8 = misc(r31 * 8);
        jn = ldsr<v2u>(a8 + c.m * 256 + (L_STM - L_MISC));
        ja = a8 + c.hf * 256 + (L_CSB - L_MISC);
        c.sc = ldsr<float>(L_STR + (32 * c.m + r31) * 4) * 1.4426950408889634f;
        if (h) jn = v2u{0, 0};
        if constexpr (isk) {
          const v4u wu = trfrag(misc((2 * c.hf + (lane & 1)) * 512 + (4 * h + (r15 >> 2)) * 32 + (2 * (g & 1) + ((lane >> 1) & 1)) * 8) + (L_WPHI - L_MISC), 256);
          const v4u z4 = {0, 0, 0, 0};
          c.wp0 = (lane & 4) ? z4 : wu;
          c.wp1 = (lane & 4) ? wu : z4;
#pragma unroll
          for (int i = 0; i < 4; ++i) mmA1(7, 2 * i);
          c.kk = mfmaB8(ldsr<v2u>(ja + 512), jn, c.kk);
        } else {
          constexpr int J0 = qdone(KA_K);
#pragma unroll
          for (int j = J0; j < 32; ++j) mmQ(j);
          c.kk = mfmaB8(ldsr<v2u>(ja), jn, c.qq);
        }
      }
      if (part == 1) {
        if constexpr (isk) { mmA1(7, 1); mmA1(7, 3); }
        c.kk = mfmaB(c.wp1, pk8(c.kk, 8), mfmaB(c.wp0, pk8(c.kk, 0), zero));
      }
      if (part == 2) {
        if constexpr (isk) {
          mmA1(7, 5);
          mmA1(7, 7);
          c.vv = mfmaB8(jn, ldsr<v2u>(ja + 1024), c.vv);
        }
        c.sa = -max16(c.kk) * c.sc;
      }
      if (part == 3) {
        c.sb = min16(c.kk) * c.sc;
        asm volatile("" : "+v"(c.sa), "+v"(c.sb), "+v"(c.sc));
        if constexpr (isk) {
          c.vb0 = pk8(c.vv, 0);
          c.vb1 = pk8(c.vv, 8);
          asm volatile("" : "+v"(c.vb0), "+v"(c.vb1));
        }
      }
    }
    if constexpr (!T && (K == KA_K + 1 || K == KA_S + 1)) {
      softmax_exp(c.kk, c.sc, c.sa, c.sb, c.se, c.sn, 4 * part);
      if (part == 3) asm volatile("" : "+v"(c.se), "+v"(c.sn));
    }
    if constexpr (!T && K == KA_S + 2) {
      if (part == 0) softmax_norm(c.se, 1.f, c.ph[0], c.ph[1]);
      if (part == 2) softmax_norm(c.sn, 1.f, c.ph[2], c.ph[3]);
      if (part == 3) asm volatile("" : "+v"(c.ph[0]), "+v"(c.ph[1]), "+v"(c.ph[2]), "+v"(c.ph[3]));
    }
    if constexpr (!T && K == KA_K + 2) {
      const unsigned tb = (KA_RED & 3) * 32768u + w * 4096;
      if (part == 0) {
        ja = misc((32 * c.m + r31) * 4);
        const unsigned ah = misc(64 * c.m + 8 * h), row = 4 * h + (r15 >> 2);
#pragma unroll
        for (int u = 0; u < 2; ++u) sdf[u] = cat(ldsr<v2u>(ah + (L_SDB - L_MISC) + 32 * u), ldsr<v2u>(ah + (L_SDB - L_MISC) + 32 * u + 16));
        c.sc = ldsr<float>(ja + (L_STR - L_MISC));
        jt = tb + row * 128 + ((((g & 1) * 2 + ((lane >> 1) & 1)) ^ row) << 4) + (lane & 1) * 8;
      }
      if (part < 2) {
        v4u p0, p1;
        softmax_norm(part ? c.sn : c.se, c.sc, p0, p1);
        const unsigned tw = tb + r31 * 128 + (((h * 2 + part * 4) ^ (r31 & 7)) << 4);
        ldsw<v4u>(tw, p0);
        ldsw<v4u>(tw ^ 16, p1);
#pragma unroll
        for (int u = 0; u < 2; ++u) tf[part & 1][u] = trfrag((jt ^ (part * 64)) + u * 2048, 1024);
      } else {
        const int sg = part & 1;
        const f16v kv = mfmaB(tf[sg][1], c.vb1, mfmaB(tf[sg][0], c.vb0, zero));
        const f16v zz = mfmaB(sdf[1], tf[sg][1], mfmaB(sdf[0], tf[sg][0], zero));
        const f8v o = (lane & 16) ? __builtin_shufflevector(kv, kv, 8, 9, 10, 11, 12, 13, 14, 15) : __builtin_shufflevector(kv, kv, 0, 1, 2, 3, 4, 5, 6, 7);
        ldsw<f4>(tb + c.vl + sg * 2048, __builtin_shufflevector(o, o, 0, 1, 2, 3));
        ldsw<f4>(tb + c.vl + sg * 2048 + 1024, __builtin_shufflevector(o, o, 4, 5, 6, 7));
        ldsw<float>(ja + c.hf * 1024 + (L_ZRED - L_MISC) + sg * 512, zz[0]);
      }
    }
  };
  {
    constexpr Due du = due<T>(K);
    constexpr Item di = item_at<T>(du.k);
    constexpr bool A1 = !T && K >= 1 && K < KA_K;
    constexpr int NKV = A1 && item_at<T>(K - 1).kind == P1 ? 8 : 0, NQ = A1 ? qdone(K + 1) - qdone(K) : 0, Q0 = qdone(K), PN = item_at<T>(K - 1).n;
    constexpr int NALL = di.kind == AA ? 16 : 24;
    constexpr int NM = (T && K >= 1 && K <= 8) ? 16 : (A1 ? NKV + NQ : (du.k >= 0 ? (du.part ? NALL / 2 : NALL) : 0));
    constexpr int M0 = du.part == 2 ? NALL / 2 : 0;
    constexpr int NR = it.kind == P1 ? 20 : (it.kind == P2 ? (T ? 24 : 32) : (it.kind == WW || it.kind == AA ? 8 : 0));
    constexpr int NS = NM < 4 ? 4 : NM;
    constexpr int RPS = NR <= (NS * 2) / 3 ? 1 : (NR <= (NS * 5) / 3 ? 2 : (NR <= NS * 2 + NS / 2 + 1 ? 3 : (NR <= NS * 4 ? 5 : 8)));
    auto mm = [&](int i) {
      if constexpr (T && K >= 1 && K <= 8) {
        constexpr int pb = (K - 1) & 1;
        c.acc[(i >> 1) & 3][i & 1] = mfma32(cat(c.wb[pb][i >> 1][0], c.wb[pb][i >> 1][1]), c.X[(K - 1) & 7][2 * (i >> 3) + (i & 1)], K == 1 && i < 8 ? z4f : c.acc[(i >> 1) & 3][i & 1]);
      } else if constexpr (A1) {
        if (i < NKV) mmA1(PN, i);
        else mmQ(Q0 + i - NKV);
      } else if constexpr (di.kind == AA) {
        const int j = M0 + i;
        c.acc2[(j >> 1) & 3][j & 1] = mfma32(c.b2[2 * di.n + (j >> 3)][j & 1], c.af[di.n & 1][j >> 1], di.n == 0 && j < 8 ? z4f : c.acc2[(j >> 1) & 3][j & 1]);
      } else if constexpr (di.kind == P2) {
        const int j = M0 + i;
        c.acc1[(j >> 1) % 3][j & 1] = mfma32(cat(c.wb[di.n & 1][j >> 1][0], c.wb[di.n & 1][j >> 1][1]), c.X[(2 * di.n + j / 12) & 7][2 * ((j / 6) & 1) + (j & 1)],
                                             di.n == 0 && j < 6 ? z4f : c.acc1[(j >> 1) % 3][j & 1]);
      }
    };
    auto rd = [&](int i) {
      if (i >= NR) return;
      if constexpr (it.kind == P1 && T) {
        if (i < 4) c.X[K & 7][i] = ldsr<v4u>(c.ax[i >> 1] + sl + (i & 1) * 2048);
        else c.wb[K & 1][(i - 4) >> 1][i & 1] = trrd(c.aw[((i - 4) >> 1) & 3] + sl + ((i - 4) >> 3) * 4096 + (i & 1) * 512);
      } else if constexpr (it.kind == P1) {
        if (i < 4) c.X[it.n][i] = ldsr<v4u>(c.ax[i] + sl);
        else c.wb[it.n & 1][(i - 4) >> 1][i & 1] = trrd(c.aw[0] + sl + 16384 + (((i - 4) >> 1) & 1) * 8192 + ((i - 4) >> 2) * 2048 + (i & 1) * 512);
      } else if constexpr (it.kind == P2 && T) {
        c.wb[it.n & 1][i >> 1][i & 1] = trrd(c.aw2[(i >> 1) % 3] + sl + (i / 12) * 16384 + ((i / 6) & 1) * 4096 + (i & 1) * 512);
      } else if constexpr (it.kind == P2) {
        c.wq[16 * it.n + (i >> 1)][i & 1] = trrd(c.aw[0] + sl + (i >> 1) * 2048 + (i & 1) * 512);
      } else if constexpr (it.kind == WW) {
        const v2u t = trrd(c.ab2[(i >> 1) & 1] + sl + (i >> 2) * 4096 + (i & 1) * 2048);
        c.b2[2 * it.n + (i >> 2)][(i >> 1) & 1][2 * (i & 1)] = t.x;
        c.b2[2 * it.n + (i >> 2)][(i >> 1) & 1][2 * (i & 1) + 1] = t.y;
      } else if constexpr (it.kind == AA) {
        c.af[it.n & 1][i] = ldsr<v4u>(c.aa[i & 3] + sl + (i >> 2) * 4096);
      }
    };
#pragma unroll
    for (int i = 0; i < NS; ++i) {
      if (i < NM) mm(i);
      if (i == NS - 4) ep_reads();
#pragma unroll
      for (int k = 0; k < RPS; ++k) rd(RPS * i + k);
      if (i % (NS / 4) == (NS > 4 ? 1 : 0) && i / (NS / 4) < 4) {
        if (K > 0) request<T, K>(c, i / (NS / 4));
        job(i / (NS / 4));
      }
      SCHED();
    }
  }
  if constexpr (EP) {
    const unsigned ob = lane * 16 + WS_A2 + ((2 * c.ti + (c.m >> 1)) * 72 + 9 * c.jj) * 4096 + (c.m & 1) * 2048;
#pragma unroll
    for (int mi = 0; mi < 2; ++mi) {
      if (g) nm[mi] = v2u{0, 0};
      v2u hp[NUN];
#pragma unroll
      for (int t = 0; t < NUN; ++t) {
        const v2u pk = pk4(mfma16(cf[t], nm[mi], K == 8 ? c.acc[t][mi] : c.acc1[t][mi]));
        hp[t] = v2u{relu2(pk.x), relu2(pk.y)};
      }
      if constexpr (K == 8) {
        const unsigned o = ob + 2 * c.hf * 4096 + mi * 1024;
        __builtin_amdgcn_raw_buffer_store_b128(cat(hp[0], hp[1]), c.rws, o, 0, 16);
        __builtin_amdgcn_raw_buffer_store_b128(cat(hp[2], hp[3]), c.rws, o + 4096, 0, 16);
      } else {
        const unsigned o = ob + 4 * 4096 + mi * 1024;
        __builtin_amdgcn_raw_buffer_store_b128(cat(hp[0], hp[1]), c.rws, o + c.hf * 8192, 0, 16);
        __builtin_amdgcn_raw_buffer_store_b64(hp[2], c.rws, o + 4096 + c.hf * 8, 0, 16);
      }
    }
    SCHED();
  }
  if constexpr (K == 0) {
    issue<T, 2>(c);
    const unsigned th = c.ti >> 3;
    const unsigned lo = T && th ? (c.jj & 1) * 2 : 0, cnt = th ? (T ? 6u : 4u) : 8u;
    if (w - lo < cnt) {
      const unsigned q = r15 >> 2;
      const unsigned a = L_GST + (w >> 2) * 8192 + (8 * g + q) * 128 + (lane & 3) * 8 + (((w & 3) ^ ((q & 2) | (g & 1))) << 5);
      const v4u ones = {0x3f803f80u, 0x3f803f80u, 0x3f803f80u, 0x3f803f80u};
      const f4 s = mfma32(ones, trfrag(a + 4096, 512), mfma32(ones, trfrag(a, 512), z4f));
      const unsigned col = T ? 1536 + 224 * c.jj + 16 * ((th ? 8 - (c.jj & 1) * 8 : (c.jj & 1) * 6) + w - lo)
                             : (th ? 0u : 512 + (w >> 2) * 512) + 64 * c.jj + (w & 3) * 16;
      if (lane < 16) __builtin_amdgcn_raw_buffer_store_b64(v2u{__float_as_uint(s.x), (unsigned)c.tag}, c.rws, WS_CS + (col + lane) * 64 + (c.ti & 7) * 8, 0, 16);
    }
    issue<T, 3>(c);
  }
  if constexpr (K == kend<T>()) { if (!c.hf) gjob<T>(c); }
}

template <int T, int K> DI void windows(St& c) {
  window<T, K>(c);
  if constexpr (K + 1 < nitems<T>()) windows<T, K + 1>(c);
}

template <int T, int K> DI void warm(St& c) {
  if constexpr (item_at<T>(K).kind == P1 || item_at<T>(K).kind == P2 || item_at<T>(K).kind == WW) { issue<T, K>(c); WAIT_VM(8); }
  if constexpr (K + 1 < nitems<T>()) warm<T, K + 1>(c);
}
template <int T> DI void program(St& c, const uint16_t* wphi, uint16_t* out) {
  constexpr unsigned WSTR = T ? 3584u : 3072u;
  const unsigned lane = c.lane, w = c.w, g = c.g, r15 = c.r15;
  u64* ts = c.ts;
  const unsigned srow = c.ti * 128 + (T * 8 + c.jj) * 8 + w;
  v4u xrow;
  asm volatile("buffer_load_dwordx4 %0, %1, %2, %3 offen" : "=v"(xrow) : "v"(c.vl), "s"(c.rx), "s"(srow * 1024) : "memory");
  c.vx0 = (c.ti * 128 + 16 * w + (lane >> 3)) * 1024u + (((lane & 7) ^ (lane >> 4)) << 4);
  c.vx1 = (c.vx0 ^ 64u) + 8192u;
  c.vw = (8 * w + (lane >> 3)) * WSTR + (T ? ((448u * c.jj) & ~127u) : c.jj * 128u) + (((lane & 7) ^ ((((lane >> 3) & 2) | (w & 1)) << 1)) << 4);
  c.sp1 = T ? (c.jj & 1) * 256 : 1024u;
  c.sp2 = T ? 256 - (c.jj & 1) * 256 : 0u;
  issue<T, 0>(c);
  {
    const unsigned th = c.ti >> 3, so = (c.ti & 7) * 64 * WSTR + (th ? c.sp2 : c.sp1);
    dma<L_GST, 0>(c.m1, c.vw, c.rw, so);
    dma<L_GST + 8192, 0>(c.m1, c.vw, c.rw, so + (T ? 128u : (th ? 0u : 1024u)));
  }
  issue<T, 1>(c);
  TS(11);
  asm volatile("s_waitcnt vmcnt(10)" : "+v"(xrow)::"memory");
  {
    float v[8];
#pragma unroll
    for (int d = 0; d < 4; ++d) { v[2 * d] = blo(xrow[d]); v[2 * d + 1] = bhi(xrow[d]); }
    const float mean = wave_add(((v[0] + v[1]) + (v[2] + v[3])) + ((v[4] + v[5]) + (v[6] + v[7]))) * (1.f / 512.f);
    float q = 0.f;
#pragma unroll
    for (int d = 0; d < 8; ++d) q = __builtin_fmaf(v[d] - mean, v[d] - mean, q);
    const float var = wave_add(q) * (1.f / 512.f);
    const float sd = __builtin_amdgcn_sqrtf(rbf(rbf(var) + 1e-5f));
    if (lane == 0) __builtin_amdgcn_raw_buffer_store_b64(v2u{pk2(mean, sd), (unsigned)c.tag}, c.rws, WS_ST + srow * 8, 0, 16);
  }
  const unsigned q = r15 >> 2;
  if (T) {
    const unsigned f = (q & 2) | (g & 1), rowp = (8 * g + q) * 128 + (lane & 3) * 8;
#pragma unroll
    for (int ks = 0; ks < 2; ++ks) c.ax[ks] = (4 * c.m + (r15 >> 3)) * 1024 + (r15 & 7) * 128 + (((4 * ks + g) ^ (r15 >> 1)) << 4);
#pragma unroll
    for (int t = 0; t < 4; ++t) c.aw[t] = 16384 + c.hf * 8192 + rowp + ((t ^ f) << 5);
    asm volatile("" : "+v"(c.ax[0]), "+v"(c.ax[1]), "+v"(c.aw[0]), "+v"(c.aw[1]), "+v"(c.aw[2]), "+v"(c.aw[3]));
  } else {
    const unsigned r31 = lane & 31, h = lane >> 5;
#pragma unroll
    for (int u = 0; u < 4; ++u) c.ax[u] = (4 * c.m + (r31 >> 3)) * 1024 + (r31 & 7) * 128 + (((2 * u + h) ^ ((r31 >> 1) & 7)) << 4);
    c.aw[0] = (8 * h + q) * 128 + (lane & 3) * 8 + (((2 * c.hf + (g & 1)) ^ ((q & 2) | h)) << 5);
    asm volatile("" : "+v"(c.ax[0]), "+v"(c.ax[1]), "+v"(c.ax[2]), "+v"(c.ax[3]), "+v"(c.aw[0]));
    ldsw<unsigned>(L_ZERO + threadIdx.x * 4, 0u);
  }

  windows<T, 0>(c);

  const unsigned nh = w & 1, kq = w >> 1;
#pragma unroll
  for (int i = 0; i < 16; ++i) c.acc2[(i >> 1) & 3][i & 1] = mfma32(c.b2[16 + (i >> 3)][i & 1], c.af[0][i >> 1], c.acc2[(i >> 1) & 3][i & 1]);
  TS(9);
  if (blockIdx.x == 0 && threadIdx.x == 0) set_flag(c.ws, WS_EPOCH, c.tag);
  constexpr unsigned rb = (((nitems<T>() - 1) & 3) < 2) ? 65536u : 0u;
#pragma unroll
  for (int s = 1; s < 4; ++s)
#pragma unroll
    for (int nf = 0; nf < 2; ++nf) ldsw<f4>(rb + ((((kq ^ s) * 2 + nh) * 3 + (s - 1)) * 2 + nf) * 1024 + lane * 16, c.acc2[s][nf]);
  WAIT_LGKM0();
  BARRIER();
  const float rs = __builtin_amdgcn_rcpf(bhi(c.gst.x));
  v2u o[2];
#pragma unroll
  for (int nf = 0; nf < 2; ++nf) {
    f4 s = c.acc2[0][nf];
#pragma unroll
    for (int k = 0; k < 3; ++k) s += ldsr<f4>(rb + ((w * 3 + k) * 2 + nf) * 1024 + lane * 16);
    o[nf] = pk4(f4{__builtin_fmaf(s.x, rs, blo(c.xr[nf].x)), __builtin_fmaf(s.y, rs, bhi(c.xr[nf].x)), __builtin_fmaf(s.z, rs, blo(c.xr[nf].y)),
                   __builtin_fmaf(s.w, rs, bhi(c.xr[nf].y))});
  }
  auto sx = __builtin_amdgcn_permlane16_swap(o[0].x, o[1].x, false, false);
  auto sy = __builtin_amdgcn_permlane16_swap(o[0].y, o[1].y, false, false);
  const unsigned col = c.c2 * 64 + nh * 32 + (g & 1) * 16 + (g >> 1) * 8;
  *(v4u*)(out + (size_t)(c.r2 * 64 + kq * 16 + r15) * 512 + col) = v4u{sx[0], sy[0], sx[1], sy[1]};
}

__global__ __launch_bounds__(512, 2) void fused_block(const uint16_t* __restrict__ x, const uint16_t* __restrict__ wqkv,
                                                      const uint16_t* __restrict__ wphi, const uint16_t* __restrict__ wo,
                                                      const uint16_t* __restrict__ w1, const uint16_t* __restrict__ w2,
                                                      uint16_t* __restrict__ out, char* __restrict__ ws) {
  __shared__ __attribute__((aligned(1024))) char smem[LDS_BYTES];
  asm volatile("" ::"s"((unsigned)(uintptr_t)(LAS char*)smem));
  u64 ts[16];
#pragma unroll
  for (int i = 0; i < 16; ++i) ts[i] = 0;
  TS(0);
  St c;
  c.ts = ts;
  c.st = 0;
  const unsigned b = blockIdx.x, xcd = b & 7, idx = b >> 3;
  c.lane = threadIdx.x & 63;
  c.w = rfl(threadIdx.x >> 6);
  {
    u64 pc;
    asm volatile("s_getpc_b64 %0" : "=s"(pc));
    if (c.w == 7) touch(mk_rsrc((const void*)(pc & ~127ull), CODE_PF), (idx * 16 + (c.lane & 15)) * 128);
  }
  c.m = c.w & 3;
  c.hf = c.w >> 2;
  c.g = c.lane >> 4;
  c.r15 = c.lane & 15;
  c.ti = (xcd & 1) * 8 + (idx >> 2);
  const unsigned type = (idx >> 1) & 1;
  c.jj = (xcd >> 1) * 2 + (idx & 1);
  c.r2 = xcd * 4 + (idx >> 3);
  c.c2 = MIX ? (idx & 7) ^ ((idx >> 2) & 2) : idx & 7;
  c.rot = ROT2 ? ((idx & 1) + ((idx >> 1) & 2) + (MIX ? (idx >> 3) & 2 : idx >> 3)) & 3 : 0;
  c.rk = ((idx >> 2) % ROTW) * (8 / ROTW);
  c.m1 = c.w * 1024;
  c.m2 = c.w * 2048;
  c.m4 = c.w * 4096;
  c.sw1 = (224 * c.w + 96 * (c.w & 1)) * 1024;
  c.sw2 = (224 * c.w + 128 - 128 * (c.w & 1)) * 1024;
  c.swo = c.w * 65536;
  c.sa2 = WS_A2 + (c.r2 * 72 + 9 * c.w) * 4096;
  c.skv = WS_KV + (c.jj * 8 + c.w) * 8192;
  c.rx = mk_rsrc(x, 2048u * 1024u);
  c.rw2 = mk_rsrc(w2, 1792u * 1024u);
  c.rwo = mk_rsrc(wo, 512u * 1024u);
  c.rws = mk_rsrc(ws, WS_END);
  c.rout = mk_rsrc(out, 2048u * 1024u);
  c.ws = ws;
  c.vl = c.lane * 16;
  c.vb2 = (c.lane >> 3) * 1024u + c.c2 * 128u + (((c.lane & 7) ^ (((c.lane >> 4) & 3) << 1)) << 4);
  c.tag = *(const __attribute__((address_space(4))) u64*)(uintptr_t)(ws + WS_EPOCH) + 1;
  if (type) {
    c.rw = mk_rsrc(w1, 512u * 3584u);
    program<1>(c, wphi, out);
  } else {
    c.rw = mk_rsrc(wqkv, 512u * 3072u);
    if (c.w < 2) dma<L_WPHI, 0>(c.m1, c.vl, mk_rsrc(wphi, 32u * 512u), c.jj * 2048 + c.m1);
    program<0>(c, wphi, out);
  }
}

extern "C" void kernel_launch(void* const* d_in, const int* in_sizes, int n_in, void* d_out, int out_size, void* d_ws,
                              size_t ws_size, hipStream_t stream) {
  fused_block<<<256, 512, 0, stream>>>((const uint16_t*)d_in[0], (const uint16_t*)d_in[1], (const uint16_t*)d_in[2],
                                       (const uint16_t*)d_in[3], (const uint16_t*)d_in[5], (const uint16_t*)d_in[6],
                                       (uint16_t*)d_out, (char*)d_ws);
}
```

```cpp
#include <hip/hip_runtime.h>
#include <stdint.h>

typedef unsigned v4u __attribute__((ext_vector_type(4)));
typedef unsigned v2u __attribute__((ext_vector_type(2)));
typedef float f4 __attribute__((ext_vector_type(4)));
typedef float f2 __attribute__((ext_vector_type(2)));
typedef float f8v __attribute__((ext_vector_type(8)));
typedef float f16v __attribute__((ext_vector_type(16)));
typedef short s8 __attribute__((ext_vector_type(8)));
typedef short s4 __attribute__((ext_vector_type(4)));
typedef short s2 __attribute__((ext_vector_type(2)));
typedef __bf16 bf2 __attribute__((ext_vector_type(2)));
typedef unsigned long long u64;
#define GAS __attribute__((address_space(1)))
#define LAS __attribute__((address_space(3)))
#define DI __device__ __forceinline__
#define RLX_AGENT __ATOMIC_RELAXED, __HIP_MEMORY_SCOPE_AGENT

#define PROFILE 0
#define MIX 0
#define ROT2 0
#define ROTW 1
#define W_PF 1
#define CODE_PF 40960

constexpr unsigned WS_EPOCH = 0;
constexpr unsigned WS_FKV = 32768;
constexpr unsigned WS_FA = 40960;
constexpr unsigned WS_ST = 16384;
constexpr unsigned WS_Z = 65536;
constexpr unsigned WS_CS = 131072;
constexpr unsigned WS_KV = 393216;
constexpr unsigned WS_A2 = WS_KV + (1u << 19);
constexpr unsigned WS_END = WS_A2 + 32u * 72u * 4096u;
constexpr unsigned WS_TS = 16u << 20;

constexpr unsigned L_GST = 131072;
constexpr unsigned L_MISC = 147456;
constexpr unsigned L_CSB = L_MISC;
constexpr unsigned L_STM = L_MISC + 2048;
constexpr unsigned L_STR = L_MISC + 3072;
constexpr unsigned L_STD = L_MISC + 3584;
constexpr unsigned L_WPHI = L_MISC + 4096;
constexpr unsigned L_ZRED = L_MISC + 6144;
constexpr unsigned L_ZERO = L_MISC + 8192;
constexpr unsigned L_KVB = L_MISC + 10240;
constexpr unsigned L_ZB = L_MISC + 14336;
constexpr unsigned L_SDB = L_MISC + 14592;
constexpr unsigned LDS_BYTES = 163840;

typedef __amdgpu_buffer_rsrc_t rsrc_t;
DI rsrc_t mk_rsrc(const void* p, unsigned bytes) { return __builtin_amdgcn_make_buffer_rsrc((void*)p, (short)0, (int)bytes, 0x00020000); }

template <unsigned MC, unsigned SC, bool SC1 = false> DI void dma(unsigned mb, unsigned voff, rsrc_t rs, unsigned sb) {
  unsigned t;
  if constexpr (SC1)
    asm volatile("s_add_u32 m0, %1, %2\n\ts_add_u32 %0, %3, %4\n\tbuffer_load_dwordx4 %5, %6, %0 offen sc1 lds"
                 : "=&s"(t) : "s"(mb), "i"(MC), "s"(sb), "i"(SC), "v"(voff), "s"(rs) : "memory", "scc");
  else
    asm volatile("s_add_u32 m0, %1, %2\n\ts_add_u32 %0, %3, %4\n\tbuffer_load_dwordx4 %5, %6, %0 offen lds"
                 : "=&s"(t) : "s"(mb), "i"(MC), "s"(sb), "i"(SC), "v"(voff), "s"(rs) : "memory", "scc");
}
DI void touch(rsrc_t rs, unsigned voff) {
  unsigned t;
  asm volatile("s_mov_b32 m0, %1\n\ts_mov_b32 %0, 0\n\tbuffer_load_dword %2, %3, %0 offen lds" : "=&s"(t) : "i"(L_KVB), "v"(voff), "s"(rs) : "memory");
}
#define WAIT_VM(n) asm volatile("s_waitcnt vmcnt(%0)" ::"i"(n) : "memory")
#define WAIT_LGKM0() do { __builtin_amdgcn_s_waitcnt(0xC07F); asm volatile("" ::: "memory"); } while (0)
#define BARRIER() asm volatile("s_barrier" ::: "memory")
#define SCHED() __builtin_amdgcn_sched_barrier(0)
#define WL(v_, l_) asm volatile("v_writelane_b32 %0, %1, %2" : "+v"(c.st) : "s"((unsigned)(v_)), "i"(l_))
#define PIN(x) asm volatile("" : "+v"(x))

DI f4 mfma32(v4u a, v4u b, f4 c) { return __builtin_amdgcn_mfma_f32_16x16x32_bf16(__builtin_bit_cast(s8, a), __builtin_bit_cast(s8, b), c, 0, 0, 0); }
DI f4 mfma16(v2u a, v2u b, f4 c) { return __builtin_amdgcn_mfma_f32_16x16x16bf16_1k(__builtin_bit_cast(s4, a), __builtin_bit_cast(s4, b), c, 0, 0, 0); }
DI f16v mfmaB(v4u a, v4u b, f16v c) { return __builtin_amdgcn_mfma_f32_32x32x16_bf16(__builtin_bit_cast(s8, a), __builtin_bit_cast(s8, b), c, 0, 0, 0); }
DI f16v mfmaB8(v2u a, v2u b, f16v c) { return __builtin_amdgcn_mfma_f32_32x32x8bf16_1k(__builtin_bit_cast(s4, a), __builtin_bit_cast(s4, b), c, 0, 0, 0); }
DI unsigned pk2(float a, float b) { bf2 r = {(__bf16)a, (__bf16)b}; return __builtin_bit_cast(unsigned, r); }
DI v2u pk4(f4 v) { return v2u{pk2(v.x, v.y), pk2(v.z, v.w)}; }
DI v4u pk8(const f16v& v, int o) { return v4u{pk2(v[o], v[o + 1]), pk2(v[o + 2], v[o + 3]), pk2(v[o + 4], v[o + 5]), pk2(v[o + 6], v[o + 7])}; }
DI unsigned relu2(unsigned p) { return __builtin_bit_cast(unsigned, __builtin_elementwise_max(__builtin_bit_cast(s2, p), s2{0, 0})); }
DI float rbf(float a) { return __uint_as_float(pk2(a, 0.f) << 16); }
DI float blo(unsigned p) { return __uint_as_float(p << 16); }
DI float bhi(unsigned p) { return __uint_as_float(p & 0xffff0000u); }
DI v2u trrd(unsigned addr) { return __builtin_bit_cast(v2u, __builtin_amdgcn_ds_read_tr16_b64_v4i16((LAS s4*)(uintptr_t)addr)); }
DI v4u cat(v2u a, v2u b) { return v4u{a.x, a.y, b.x, b.y}; }
DI v4u trfrag(unsigned addr, unsigned off2) { return cat(trrd(addr), trrd(addr + off2)); }
template <class T> DI T ldsr(unsigned addr) { return *(LAS T*)(uintptr_t)addr; }
template <class T> DI void ldsw(unsigned addr, T v) { *(LAS T*)(uintptr_t)addr = v; }
DI unsigned rfl(unsigned x) { return __builtin_amdgcn_readfirstlane(x); }
DI float max3(float a, float b, float c) { return __builtin_elementwise_maximum(__builtin_elementwise_maximum(a, b), c); }
DI float min3(float a, float b, float c) { return __builtin_elementwise_minimum(__builtin_elementwise_minimum(a, b), c); }
template <int CTRL> DI float dpp_add(float x) { return x + __builtin_bit_cast(float, __builtin_amdgcn_update_dpp(0, __builtin_bit_cast(int, x), CTRL, 0xf, 0xf, false)); }

DI float xg_add(float x) {
  auto a = __builtin_amdgcn_permlane16_swap(__float_as_uint(x), __float_as_uint(x), false, false);
  x = __uint_as_float(a[0]) + __uint_as_float(a[1]);
  auto b = __builtin_amdgcn_permlane32_swap(__float_as_uint(x), __float_as_uint(x), false, false);
  return __uint_as_float(b[0]) + __uint_as_float(b[1]);
}
DI float wave_add(float x) { return xg_add(dpp_add<0x121>(dpp_add<0x122>(dpp_add<0x124>(dpp_add<0x128>(x))))); }
DI float sum16(const f16v& v) {
  const f8v a = __builtin_shufflevector(v, v, 0, 1, 2, 3, 4, 5, 6, 7) + __builtin_shufflevector(v, v, 8, 9, 10, 11, 12, 13, 14, 15);
  const f4 b = __builtin_shufflevector(a, a, 0, 1, 2, 3) + __builtin_shufflevector(a, a, 4, 5, 6, 7);
  const f2 c = __builtin_shufflevector(b, b, 0, 1) + __builtin_shufflevector(b, b, 2, 3);
  return c.x + c.y;
}
DI float max16(const f16v& p) {
  return max3(max3(max3(p[0], p[1], p[2]), max3(p[3], p[4], p[5]), max3(p[6], p[7], p[8])),
              max3(max3(p[9], p[10], p[11]), max3(p[12], p[13], p[14]), p[15]), p[15]);
}
DI float min16(const f16v& p) {
  return min3(min3(min3(p[0], p[1], p[2]), min3(p[3], p[4], p[5]), min3(p[6], p[7], p[8])),
              min3(min3(p[9], p[10], p[11]), min3(p[12], p[13], p[14]), p[15]), p[15]);
}
DI void softmax_exp(const f16v& p, float sc, float a, float b, f16v& e, f16v& n, int r0) {
#pragma unroll
  for (int r = r0; r < r0 + 4; r += 2) {
    const f2 x = {p[r], p[r + 1]};
    const f2 u = __builtin_elementwise_fma(x, f2{sc, sc}, f2{a, a}), v = __builtin_elementwise_fma(x, f2{-sc, -sc}, f2{b, b});
    e[r] = __builtin_amdgcn_exp2f(u.x);
    e[r + 1] = __builtin_amdgcn_exp2f(u.y);
    n[r] = __builtin_amdgcn_exp2f(v.x);
    n[r + 1] = __builtin_amdgcn_exp2f(v.y);
  }
}
DI void softmax_norm(f16v e, float os, v4u& o0, v4u& o1) {
  e *= __builtin_amdgcn_rcpf(sum16(e)) * os;
  o0 = pk8(e, 0);
  o1 = pk8(e, 8);
}

constexpr unsigned SPIN_CAP = 1u << 18;
DI void set_flag(char* ws, unsigned off, u64 tag) { __hip_atomic_store((GAS u64*)(ws + off), tag, RLX_AGENT); }

#define TS(k)

enum Kind { P1, P2, WW, AA, KV, RED, END };
struct Item { int kind, n, ld = 3; };
#define SEQ_B {P2,0},{WW,0},{P2,1},{WW,1},{P2,2},{WW,2},{P2,3},{WW,3},{WW,4},{AA,0},{WW,5},{AA,1},{WW,6},{AA,2},{WW,7},{AA,3},{WW,8},{AA,4},{AA,5},{AA,6},{AA,7,2},{AA,8}
#define SEQ_A {WW,3},{WW,4},{WW,5},{AA,0},{WW,6},{KV,0,2},{KV,1},{AA,1},{WW,7},{AA,2},{WW,8},{AA,3},{AA,4},{AA,5},{AA,6},{AA,7,2},{AA,8}
constexpr Item seqB[] = {{P1,0},{P1,1},{P1,2},{P1,3},{P1,4},{P1,5},{P1,6},{P1,7}, SEQ_B};
constexpr Item seqA[] = {{P1,0},{P1,1},{P2,0},{P1,2},{P1,3},{P1,4},{P2,1},{P1,5},{P1,6},{P1,7},{WW,0},{WW,1},{WW,2},{RED,0}, SEQ_A};
template <int T> constexpr int nitems() { return T ? (int)(sizeof(seqB) / sizeof(Item)) : (int)(sizeof(seqA) / sizeof(Item)); }
template <int T> constexpr Item item_at(int k) { return k < 0 || k >= nitems<T>() ? Item{END, 0} : (T ? seqB[k] : seqA[k]); }
template <int T> constexpr int nops(int k) { return item_at<T>(k).kind == END || item_at<T>(k).kind == RED ? 0 : 4; }
template <int T> constexpr int find(int kind, int n) {
  for (int k = 0; k < nitems<T>(); ++k)
    if (item_at<T>(k).kind == kind && item_at<T>(k).n == n) return k;
  return -100;
}
template <int T> constexpr int iw(int k) {
  int w = -1;
  for (int j = 2; j <= k; ++j) {
    const int x = j - item_at<T>(j).ld;
    w = x > w ? x : (w < 0 ? 0 : w);
  }
  return w;
}
template <int T> constexpr int first_req(int W) {
  for (int k = 0; k < nitems<T>(); ++k)
    if (iw<T>(k) == W && nops<T>(k)) return k;
  return 1000;
}
template <int T> constexpr int nreq(int W) {
  int n = 0;
  for (int k = 0; k < nitems<T>(); ++k)
    if (iw<T>(k) == W) n += nops<T>(k);
  return n;
}
template <int T> constexpr int gate_of(int k) {
  const Item it = item_at<T>(k);
  if (it.kind == KV && it.n == 0) return 3;
  return it.kind == AA ? (it.n == 0 ? 0 : (it.n == 4 ? 1 : (it.n == 7 ? 2 : -1))) : -1;
}
#define LEAD_KV 1
#define LEAD_AA 2
constexpr int lead(int gt) { return gt == 3 ? LEAD_KV : LEAD_AA; }
template <int T> constexpr int gate_in(int W) {
  for (int k = 0; k < nitems<T>(); ++k)
    if (iw<T>(k) == W && gate_of<T>(k) >= 0) return gate_of<T>(k);
  return -1;
}
template <int T> constexpr bool lagged(int k) { return item_at<T>(k).kind == AA || (T && item_at<T>(k).kind == P2); }
template <int T> constexpr bool split(int k) { return lagged<T>(k) && !lagged<T>(k + 1) && item_at<T>(k + 2).kind != END; }
struct Due { int k, part; };
template <int T> constexpr Due due(int K) {
  if (lagged<T>(K - 1)) return Due{K - 1, split<T>(K - 1) ? 1 : 0};
  if (split<T>(K - 2)) return Due{K - 2, 2};
  return Due{-1, 0};
}
constexpr int KB_E2 = find<1>(P2, 3) + (split<1>(find<1>(P2, 3)) ? 2 : 1);
constexpr int KA_K = find<0>(P1, 7) + 1, KA_RED = find<0>(RED, 0), KA_S = KA_RED + 1, KA_O = find<0>(KV, 1);
static_assert(KA_RED == KA_K + 3, "");
template <int T> constexpr int kend() { return find<T>(P1, 7); }
#define QCAP 12
constexpr int qdone(int K) {
  int d = 0;
  for (int k = 1; k < K && k < KA_K; ++k) {
    int room = QCAP - (item_at<0>(k - 1).kind == P1 ? 8 : 0);
    while (room > 0 && d < 32 && find<0>(P1, d >> 2) < k && find<0>(P2, d >> 4) < k) { ++d; --room; }
  }
  return d;
}
struct Ev { int win, n, after, fw; };
#define FW_H1 3
#define FW_H2 3
#define FW_KV 2
#define FW_O 3
template <int T> constexpr Ev ev(int i) {
  return T ? (i ? Ev{KB_E2, 4, 1, KB_E2 + FW_H2} : Ev{8, 4, 1, 8 + FW_H1}) : (i ? Ev{KA_O, 2, 0, KA_O + FW_O} : Ev{KA_RED, 2, 0, KA_RED + FW_KV});
}
template <int T> constexpr int allowed(int K) {
  if (K == 0) return 5;
  const int need = item_at<T>(K).kind == KV && item_at<T>(K).n == 0 ? K + 1 : K;
  int n = K == 1 ? 1 : 0;
  for (int k = need + 1; k < nitems<T>(); ++k)
    if (iw<T>(k) < K) n += nops<T>(k);
  for (int i = 0; i < 2; ++i) {
    const Ev e = ev<T>(i);
    if (e.win < K && e.win + e.after > iw<T>(need)) n += e.n;
  }
  if (!nops<T>(need)) n = 63;
  for (int i = 0; i < 2; ++i) {
    const Ev e = ev<T>(i);
    if (K == e.fw) {
      int y = 0;
      for (int k = e.win + e.after; k < K; ++k) y += nreq<T>(k);
      if (y < n) n = y;
    }
  }
  return n;
}

struct St {
  rsrc_t rx, rw, rw2, rwo, rws, rout;
  char* ws;
  u64 tag;
  unsigned lane, w, m, hf, g, r15, ti, jj, r2, c2, rot, rk;
  unsigned m1, m2, m4;
  unsigned sp1, sp2, sw1, sw2, swo, sa2, skv;
  unsigned vx0, vx1, vw, vb2, vl;
  unsigned ax[4], aw[4], aw2[3], ab2[2], aa[4];
  v4u X[8][4];
  v2u wb[2][12][2], wq[32][2];
  f4 acc[4][2], acc1[3][2];
  f16v kk, vv, qq;
  v4u wp0, wp1, ph[4];
  f16v se, sn;
  v4u vb0, vb1;
  float sa, sb, sc;
  v4u b2[18][2], af[2][8];
  f4 acc2[4][2];
  v2u gate[4], xr[2], gst;
  u64* ts;
  u64 t0, t1;
  unsigned st;
};

template <int n> DI unsigned phys(const St& c) {
  if (n < 4) return (n + c.rot) & 3;
  if (n >= 7) return (n - 7 + c.rot) & 1;
  const unsigned t = n - 4 + c.rot;
  return t >= 3 ? t - 3 : t;
}
template <int T, int K, int p> DI void issue_piece(St& c) {
  constexpr Item it = item_at<T>(K);
  constexpr unsigned sl = (K & 3) * 32768u, WSTR = T ? 3584u : 3072u, n = it.n;
  if constexpr (it.kind == P1) {
    const unsigned kc = (n + c.rk) & 7;
    if constexpr (p < 2) dma<sl + p * 1024, 0>(c.m2, p ? c.vx1 : c.vx0, c.rx, kc * 128);
    else dma<sl + 16384 + (p - 2) * 8192, (p - 2) * (T ? 128 : 1024)>(c.m1, c.vw, c.rw, c.sp1 + kc * 64 * WSTR);
  } else if constexpr (it.kind == P2) {
    dma<sl + p * 8192, (T ? (p & 1) * 128 : 0)>(c.m1, c.vw, c.rw, c.sp2 + (((T ? 2 * n + (p >> 1) : 4 * n + p) + c.rk) & 7) * 64 * WSTR);
  } else if constexpr (it.kind == WW) {
    if constexpr (n < 4) dma<sl + p * 1024, 8 * p * 1024>(c.m4, c.vb2, c.rw2, c.sw1 + phys<n>(c) * 32768);
    else if constexpr (n < 7) dma<sl + p * 1024, 8 * p * 1024>(c.m4, c.vb2, c.rw2, c.sw2 + phys<n>(c) * 32768);
    else dma<sl + p * 1024, (16 * (p & 1) + 8 * (p >> 1)) * 1024>(c.m4, c.vb2, c.rwo, c.swo + phys<n>(c) * 32768);
  } else if constexpr (it.kind == AA) {
    dma<sl + p * 1024, (n < 4 ? 0 : (n < 7 ? 4 : 7)) * 4096 + p * 1024, true>(c.m4, c.vl, c.rws, c.sa2 + phys<n>(c) * 4096);
  } else if constexpr (it.kind == KV) {
    dma<sl + p * 1024, n * 256 + p * 2048, true>(c.m4, c.g * 512 + ((c.r15 ^ (c.g << 1)) << 4), c.rws, c.skv);
  }
}
template <int T, int K> DI void issue_rt(St& c, int p) {
  if (p == 0) issue_piece<T, K, 0>(c);
  if (p == 1) issue_piece<T, K, 1>(c);
  if (p == 2) issue_piece<T, K, 2>(c);
  if (p == 3) issue_piece<T, K, 3>(c);
}
template <int T, int W> DI void request(St& c, int q) {
  constexpr int k = first_req<T>(W), n = nreq<T>(W);
  static_assert(n == 0 || n == 4 || (n == 8 && nops<T>(k + 1) == 4 && iw<T>(k + 1) == W), "");
  if constexpr (n == 4) issue_rt<T, k>(c, q);
  if constexpr (n == 8) {
    if (q < 2) { issue_rt<T, k>(c, 2 * q); issue_rt<T, k>(c, 2 * q + 1); }
    else { issue_rt<T, k + 1>(c, 2 * q - 4); issue_rt<T, k + 1>(c, 2 * q - 3); }
  }
}
template <int T, int K> DI void issue(St& c) {
  issue_piece<T, K, 0>(c);
  issue_piece<T, K, 1>(c);
  issue_piece<T, K, 2>(c);
  issue_piece<T, K, 3>(c);
}
DI void snapshot(St& c, v2u& r, unsigned addr) { asm volatile("buffer_load_dwordx2 %0, %1, %2, 0 offen sc1" : "=v"(r) : "v"(addr), "s"(c.rws) : "memory"); }
template <int N> DI void gate(St& c, v2u& r, unsigned addr) {
  asm volatile("s_waitcnt vmcnt(%1)" : "+v"(r) : "i"(N) : "memory");
  if (!__all(r.x == (unsigned)c.tag && r.y == (unsigned)(c.tag >> 32))) {
    GAS u64* p = (GAS u64*)(c.ws + addr);
#pragma unroll 1
    for (unsigned spins = 0; spins < SPIN_CAP; ++spins) {
      if (__all(__hip_atomic_load(p, RLX_AGENT) == c.tag)) break;
      __builtin_amdgcn_s_sleep(2);
    }
  }
  asm volatile("" ::: "memory");
}
DI unsigned gate_addr(const St& c, int gt) {
  return gt == 3 ? WS_FKV + (c.jj * 8 + c.w) * 128 + c.r15 * 8
                 : WS_FA + (((c.r2 >> 1) * 3 + gt) * 8 + c.w) * 64 + (2 * (c.r2 & 1) + (c.lane & 1) + 2 * (c.lane & 2)) * 8;
}
DI unsigned misc(unsigned lanepart) {
  unsigned a = L_MISC + lanepart;
  PIN(a);
  return a;
}
template <int T> DI void gload(St& c) {
  const unsigned col = T ? 1536 + 224 * c.jj + 32 * c.w : (c.w < 6 ? c.w >> 1 : 2u) * 512 + 64 * c.jj + 32 * (c.w & 1);
  dma<L_GST, WS_CS, true>(c.m2, c.vl, c.rws, col * 64);
  dma<L_GST + 1024, WS_CS + 1024, true>(c.m2, c.vl, c.rws, col * 64);
  if (c.w < 2) dma<L_ZRED, WS_ST, true>(c.m1, c.vl, c.rws, c.ti * 1024);
}
template <int T> DI void gjob(St& c) {
  const unsigned lane = c.lane, w = c.w, tg = (unsigned)c.tag;
  v4u g0, g1;
  v2u gs;
#pragma unroll 1
  for (unsigned spins = 0; spins < SPIN_CAP; ++spins) {
    g0 = ldsr<v4u>(L_GST + threadIdx.x * 32);
    g1 = ldsr<v4u>(L_GST + threadIdx.x * 32 + 16);
    gs = ldsr<v2u>(L_ZRED + (w & 1) * 1024 + (threadIdx.x & 127) * 8);
    const bool ok = ((g0.y == tg && g0.w == tg && g1.y == tg && g1.w == tg) || (T && w == 7)) && (gs.y == tg || w >= 2);
    if (__all(ok)) break;
    gload<T>(c);
    WAIT_VM(0);
  }
  const float s = dpp_add<0xB1>((__uint_as_float(g0.x) + __uint_as_float(g0.z)) + (__uint_as_float(g1.x) + __uint_as_float(g1.z)));
  const float hi = rbf(s), mid = rbf(s - hi);
  if (!(lane & 1)) ldsw<v2u>(L_CSB + (threadIdx.x >> 1) * 8, v2u{pk2(hi, mid), pk2((s - hi) - mid, 0.f)});
  if (w < 2) {
    const unsigned nm = (gs.x & 0xffffu) ^ 0x8000u;
    ldsw<v2u>(L_STM + threadIdx.x * 8, v2u{nm | (nm << 16), nm});
    if (!T) {
      ldsw<float>(L_STR + threadIdx.x * 4, __builtin_amdgcn_rcpf(bhi(gs.x)));
      ldsw<float>(L_STD + threadIdx.x * 4, bhi(gs.x));
      ldsw<uint16_t>(L_SDB + threadIdx.x * 2, (uint16_t)(gs.x >> 16));
    }
  }
  SCHED();
}

template <int T, int K> DI void window(St& c) {
  constexpr Item it = item_at<T>(K);
  constexpr unsigned sl = (K & 1) * 32768u;
  constexpr unsigned hi = (K & 2) * 32768u, sla = sl + hi;
  constexpr int KW = find<T>(WW, 0), KA = find<T>(AA, 0);
  const unsigned lane = c.lane, w = c.w, g = c.g, r15 = c.r15, r31 = lane & 31, h = lane >> 5;
  u64* ts = c.ts;
  WAIT_LGKM0();
  constexpr int alw = allowed<T>(K);
  WAIT_VM(alw);
  if constexpr (K == ev<T>(0).fw) { if (lane == 0) set_flag(c.ws, T ? WS_FA + (c.ti * 24 + c.jj) * 64 + w * 8 : WS_FKV + (c.jj * 8 + w) * 128 + c.ti * 8, c.tag); TS(3); }
  if constexpr (K == ev<T>(1).fw) { if (lane == 0) set_flag(c.ws, WS_FA + (c.ti * 24 + (T ? 8 : 16) + c.jj) * 64 + w * 8, c.tag); TS(T ? 4 : 6); }
  BARRIER();
  SCHED();
  if constexpr (K == 0) TS(12);
  if constexpr (K == kend<T>() + 1) TS(1);
  if constexpr (K == (T ? KB_E2 : KA_RED)) TS(2);
  if constexpr (it.kind == AA && it.n == 0) TS(7);
  if constexpr (it.kind == AA && it.n == 4) TS(8);
  if constexpr (it.kind == AA && it.n == 7) TS(13);
  if constexpr (it.kind == KV && it.n == 0) TS(5);
  if constexpr (gate_in<T>(K) >= 0) {
    constexpr int gt = gate_in<T>(K);
    gate<nreq<T>(K - 1) + (lead(gt) > 1 ? nreq<T>(K - 2) : 0)>(c, c.gate[gt], gate_addr(c, gt));
    if constexpr (gt == 0 && T) TS(5);
    if constexpr (gt == 1 && T) TS(6);
    if constexpr (gt == 2) TS(14);
    if constexpr (gt == 3) {
      TS(4);
      dma<L_GST, WS_Z, true>(c.m1, r15 * 64 + g * 16, c.rws, (c.jj * 8 + w) * 1024);
    }
  }
  constexpr int g1 = gate_in<T>(K + 1), g2 = gate_in<T>(K + 2);
  if constexpr (g1 >= 0 && lead(g1) == 1) snapshot(c, c.gate[g1], gate_addr(c, g1));
  if constexpr (g2 >= 0 && lead(g2) == 2) snapshot(c, c.gate[g2], gate_addr(c, g2));
  if constexpr (K == 1) {
    if (w == 6) touch(c.rw2, blockIdx.x * 7168 + (lane < 56 ? lane : 0) * 128);
    if (w == 5) touch(c.rwo, blockIdx.x * 2048 + r15 * 128);
    if (w == 4) touch(c.rout, (c.r2 * 64 + lane) * 1024 + c.c2 * 128);
  }
  if constexpr (K == kend<T>() - 2) gload<T>(c);
  if constexpr (K == nitems<T>() - 5) {
    const unsigned row = c.r2 * 64 + (w >> 1) * 16 + r15;
    const unsigned xo = row * 1024 + (c.c2 * 64 + (w & 1) * 32 + 4 * g) * 2;
    c.xr[0] = __builtin_amdgcn_raw_buffer_load_b64(c.rx, xo, 0, 0);
    c.xr[1] = __builtin_amdgcn_raw_buffer_load_b64(c.rx, xo + 32, 0, 0);
    c.gst = __builtin_amdgcn_raw_buffer_load_b64(c.rws, WS_ST + row * 8, 0, 16);
    asm volatile("" ::: "memory");
  }
  if constexpr (K > 0 && !(K & 1)) {
    if constexpr (K <= kend<T>()) {
#pragma unroll
      for (int t = 0; t < (T ? 2 : 4); ++t) { c.ax[t] ^= 65536u; PIN(c.ax[t]); }

#pragma unroll
      for (int t = 0; t < (T ? 4 : 1); ++t) { c.aw[t] ^= 65536u; PIN(c.aw[t]); }
    }
    if constexpr (T && K > 8 && K <= find<1>(P2, 3)) {
#pragma unroll
      for (int t = 0; t < 3; ++t) { c.aw2[t] ^= 65536u; PIN(c.aw2[t]); }
    }
    if constexpr (K > KW && K <= find<T>(WW, 8)) {
#pragma unroll
      for (int t = 0; t < 2; ++t) { c.ab2[t] ^= 65536u; PIN(c.ab2[t]); }
    }
    if constexpr (K > KA) {
#pragma unroll
      for (int t = 0; t < 4; ++t) { c.aa[t] ^= 65536u; PIN(c.aa[t]); }
    }
  }
  if constexpr (T && K == 8) {
    const unsigned q = r15 >> 2;
#pragma unroll
    for (int t = 0; t < 3; ++t) {
      const unsigned lu = (c.jj & 1) * 2 + (t < 2 ? 4 * c.hf + t : 2 + c.hf);
      c.aw2[t] = (lu >> 2) * 8192 + (8 * g + q) * 128 + (lane & 3) * 8 + (((lu & 3) ^ ((q & 2) | (g & 1))) << 5);
    }
  }
  if constexpr (K == KW) {
    const unsigned q = r15 >> 2;
#pragma unroll
    for (int nf = 0; nf < 2; ++nf) { c.ab2[nf] = hi + (w >> 1) * 8192 + (4 * g + q) * 128 + (((2 * (w & 1) + nf) ^ ((q >> 1) | ((g & 1) << 1))) << 5) + (lane & 3) * 8; PIN(c.ab2[nf]); }
  }
  if constexpr (K == KA) {
#pragma unroll
    for (int s = 0; s < 4; ++s) { c.aa[s] = hi + (w >> 1) * 8192 + ((s ^ (w >> 1)) << 10) + lane * 16; PIN(c.aa[s]); }
  }
  const f4 z4f = {0.f, 0.f, 0.f, 0.f};
  f16v zero;
#pragma unroll
  for (int r = 0; r < 16; ++r) zero[r] = 0.f;

  if constexpr (K == kend<T>()) { if (c.hf) gjob<T>(c); }
  if constexpr (!T && K == KA_RED) {
    const unsigned ra = sla + (w & 4) * 4096 + (w & 3) * 1024 + lane * 16;
    const unsigned za = misc((w >> 1) * 512 + (16 * (r15 >> 3) + 8 * (w & 1) + (r15 & 7)) * 4) + (L_ZRED - L_MISC);
    f4 s = ldsr<f4>(ra);
    float z = ldsr<float>(za);
#pragma unroll
    for (int k = 1; k < 4; ++k) {
      s += ldsr<f4>(ra + k * 4096);
      z += ldsr<float>(za + k * 128);
    }
    __builtin_amdgcn_raw_buffer_store_b64(pk4(s), c.rws, lane * 8 + WS_KV + ((c.jj * 8 + w) * 16 + c.ti) * 512, 0, 16);
    if (lane < 16) __builtin_amdgcn_raw_buffer_store_b32(__float_as_uint(z), c.rws, WS_Z + (c.jj * 8 + w) * 1024 + lane * 64 + c.ti * 4, 0, 16);
    SCHED();
  }
  if constexpr (it.kind == KV && it.n == 0) {
    const unsigned q = r15 >> 2, a = w * 4096 + (4 * g + q) * 256 + (lane & 3) * 8;
    f4 s[2] = {z4f, z4f};
#pragma unroll
    for (int b = 0; b < 16; ++b) {
      const unsigned sel = r15 == (unsigned)b ? 0x3f803f80u : 0u;
      s[b & 1] = mfma16(trrd(((K + (b >> 3)) & 3) * 32768u + a + (((b & 3) ^ q) << 5) + ((b >> 2) & 1) * 128), v2u{sel, sel}, s[b & 1]);
    }
    const f4 zp = ldsr<f4>(L_GST + w * 1024 + lane * 16);
    const float z = xg_add((zp.x + zp.y) + (zp.z + zp.w));
    const unsigned L = 4 * r15 + g;
    ldsw<v2u>(L_KVB + (w * 32 + (L & 31)) * 16 + (L >> 5) * 8, pk4(s[0] + s[1]));
    if (lane < 16) ldsw<uint16_t>(L_ZB + w * 32 + lane * 2, (uint16_t)pk2(z, 0.f));
    SCHED();
  }
  if constexpr (!T && K == KA_O) {
    const bool on = (r31 >> 4) == h;
    const unsigned ka = on ? L_KVB + (c.hf * 128 + r31) * 16 : L_ZERO, za = on ? L_ZB + c.hf * 128 + h * 16 : L_ZERO;
    f16v num = zero, den = zero;
#pragma unroll
    for (int t = 0; t < 4; ++t) {
      num = mfmaB(ldsr<v4u>(ka + t * 512), c.ph[t], num);
      den = mfmaB(ldsr<v4u>(za + t * 32), c.ph[t], den);
    }
    const float sd = ldsr<float>(L_STD + (32 * c.m + r31) * 4);
    const float r0 = __builtin_amdgcn_rcpf(den[0] + 1e-6f) * sd, r1 = __builtin_amdgcn_rcpf(den[8] + 1e-6f) * sd;
#pragma unroll
    for (int r = 0; r < 16; ++r) num[r] *= r < 8 ? r0 : r1;
    const unsigned ob = WS_A2 + ((2 * c.ti + (c.m >> 1)) * 72 + 9 * c.jj + 7 + c.hf) * 4096 + (2 * (c.m & 1) + (r31 >> 4)) * 1024 + (r15 + 16 * h) * 16;
    __builtin_amdgcn_raw_buffer_store_b128(pk8(num, 0), c.rws, ob, 0, 16);
    __builtin_amdgcn_raw_buffer_store_b128(pk8(num, 8), c.rws, ob + 512, 0, 16);
    SCHED();
  }
  constexpr bool EP = T && (K == 8 || K == KB_E2);
  constexpr int NUN = K == 8 ? 4 : 3;
  v2u cf[4], nm[2];
  auto ep_reads = [&]() {
  if constexpr (EP) {
    const unsigned ca = misc((((K == 8 ? (c.jj & 1) * 6 : 8 - (c.jj & 1) * 8) + 4 * c.hf) * 16 + r15) * 8), na = misc((32 * c.m + r15) * 8);
#pragma unroll
    for (int t = 0; t < NUN; ++t) cf[t] = ldsr<v2u>(ca + (L_CSB - L_MISC) + t * 128 - (K != 8 && t == 2 ? c.hf * 384 : 0));
#pragma unroll
    for (int mi = 0; mi < 2; ++mi) nm[mi] = ldsr<v2u>(na + (L_STM - L_MISC) + mi * 128);
  }
  };
  auto mmA1 = [&](int kc, int i) {
    const v4u wf = cat(c.wb[kc & 1][i][0], c.wb[kc & 1][i][1]), xf = c.X[kc][i >> 1];
    if (i & 1) c.vv = mfmaB(xf, wf, kc == 0 && i < 2 ? zero : c.vv);
    else c.kk = mfmaB(wf, xf, kc == 0 && i < 2 ? zero : c.kk);
  };
  auto mmQ = [&](int j) { c.qq = mfmaB(cat(c.wq[j][0], c.wq[j][1]), c.X[j >> 2][j & 3], j == 0 ? zero : c.qq); };
  v2u jn;
  unsigned ja, jt;
  v4u tf[2][2], sdf[2];
  auto job = [&](int part) {
    if constexpr (!T && (K == KA_K || K == KA_S)) {
      constexpr bool isk = K == KA_K;
      if (part == 0) {
        const unsigned a8 = misc(r31 * 8);
        jn = ldsr<v2u>(a8 + c.m * 256 + (L_STM - L_MISC));
        ja = a8 + c.hf * 256 + (L_CSB - L_MISC);
        c.sc = ldsr<float>(L_STR + (32 * c.m + r31) * 4) * 1.4426950408889634f;
        if (h) jn = v2u{0, 0};
        if constexpr (isk) {
          const v4u wu = trfrag(misc((2 * c.hf + (lane & 1)) * 512 + (4 * h + (r15 >> 2)) * 32 + (2 * (g & 1) + ((lane >> 1) & 1)) * 8) + (L_WPHI - L_MISC), 256);
          const v4u z4 = {0, 0, 0, 0};
          c.wp0 = (lane & 4) ? z4 : wu;
          c.wp1 = (lane & 4) ? wu : z4;
#pragma unroll
          for (int i = 0; i < 4; ++i) mmA1(7, 2 * i);
          c.kk = mfmaB8(ldsr<v2u>(ja + 512), jn, c.kk);
        } else {
          constexpr int J0 = qdone(KA_K);
#pragma unroll
          for (int j = J0; j < 32; ++j) mmQ(j);
          c.kk = mfmaB8(ldsr<v2u>(ja), jn, c.qq);
        }
      }
      if (part == 1) {
        if constexpr (isk) { mmA1(7, 1); mmA1(7, 3); }
        c.kk = mfmaB(c.wp1, pk8(c.kk, 8), mfmaB(c.wp0, pk8(c.kk, 0), zero));
      }
      if (part == 2) {
        if constexpr (isk) {
          mmA1(7, 5);
          mmA1(7, 7);
          c.vv = mfmaB8(jn, ldsr<v2u>(ja + 1024), c.vv);
        }
        c.sa = -max16(c.kk) * c.sc;
      }
      if (part == 3) {
        c.sb = min16(c.kk) * c.sc;
        asm volatile("" : "+v"(c.sa), "+v"(c.sb), "+v"(c.sc));
        if constexpr (isk) {
          c.vb0 = pk8(c.vv, 0);
          c.vb1 = pk8(c.vv, 8);
          asm volatile("" : "+v"(c.vb0), "+v"(c.vb1));
        }
      }
    }
    if constexpr (!T && (K == KA_K + 1 || K == KA_S + 1)) {
      softmax_exp(c.kk, c.sc, c.sa, c.sb, c.se, c.sn, 4 * part);
      if (part == 3) asm volatile("" : "+v"(c.se), "+v"(c.sn));
    }
    if constexpr (!T && K == KA_S + 2) {
      if (part == 0) softmax_norm(c.se, 1.f, c.ph[0], c.ph[1]);
      if (part == 2) softmax_norm(c.sn, 1.f, c.ph[2], c.ph[3]);
      if (part == 3) asm volatile("" : "+v"(c.ph[0]), "+v"(c.ph[1]), "+v"(c.ph[2]), "+v"(c.ph[3]));
    }
    if constexpr (!T && K == KA_K + 2) {
      const unsigned tb = (KA_RED & 3) * 32768u + w * 4096;
      if (part == 0) {
        ja = misc((32 * c.m + r31) * 4);
        const unsigned ah = misc(64 * c.m + 8 * h), row = 4 * h + (r15 >> 2);
#pragma unroll
        for (int u = 0; u < 2; ++u) sdf[u] = cat(ldsr<v2u>(ah + (L_SDB - L_MISC) + 32 * u), ldsr<v2u>(ah + (L_SDB - L_MISC) + 32 * u + 16));
        c.sc = ldsr<float>(ja + (L_STR - L_MISC));
        jt = tb + row * 128 + ((((g & 1) * 2 + ((lane >> 1) & 1)) ^ row) << 4) + (lane & 1) * 8;
      }
      if (part < 2) {
        v4u p0, p1;
        softmax_norm(part ? c.sn : c.se, c.sc, p0, p1);
        const unsigned tw = tb + r31 * 128 + (((h * 2 + part * 4) ^ (r31 & 7)) << 4);
        ldsw<v4u>(tw, p0);
        ldsw<v4u>(tw ^ 16, p1);
#pragma unroll
        for (int u = 0; u < 2; ++u) tf[part & 1][u] = trfrag((jt ^ (part * 64)) + u * 2048, 1024);
      } else {
        const int sg = part & 1;
        const f16v kv = mfmaB(tf[sg][1], c.vb1, mfmaB(tf[sg][0], c.vb0, zero));
        const f16v zz = mfmaB(sdf[1], tf[sg][1], mfmaB(sdf[0], tf[sg][0], zero));
        const f8v o = (lane & 16) ? __builtin_shufflevector(kv, kv, 8, 9, 10, 11, 12, 13, 14, 15) : __builtin_shufflevector(kv, kv, 0, 1, 2, 3, 4, 5, 6, 7);
        ldsw<f4>(tb + c.vl + sg * 2048, __builtin_shufflevector(o, o, 0, 1, 2, 3));
        ldsw<f4>(tb + c.vl + sg * 2048 + 1024, __builtin_shufflevector(o, o, 4, 5, 6, 7));
        ldsw<float>(ja + c.hf * 1024 + (L_ZRED - L_MISC) + sg * 512, zz[0]);
      }
    }
  };
  {
    constexpr Due du = due<T>(K);
    constexpr Item di = item_at<T>(du.k);
    constexpr bool A1 = !T && K >= 1 && K < KA_K;
    constexpr int NKV = A1 && item_at<T>(K - 1).kind == P1 ? 8 : 0, NQ = A1 ? qdone(K + 1) - qdone(K) : 0, Q0 = qdone(K), PN = item_at<T>(K - 1).n;
    constexpr int NALL = di.kind == AA ? 16 : 24;
    constexpr int NM = (T && K >= 1 && K <= 8) ? 16 : (A1 ? NKV + NQ : (du.k >= 0 ? (du.part ? NALL / 2 : NALL) : 0));
    constexpr int M0 = du.part == 2 ? NALL / 2 : 0;
    constexpr int NR = it.kind == P1 ? 20 : (it.kind == P2 ? (T ? 24 : 32) : (it.kind == WW || it.kind == AA ? 8 : 0));
    constexpr int NS = NM < 4 ? 4 : NM;
    constexpr int RPS = NR <= (NS * 2) / 3 ? 1 : (NR <= (NS * 5) / 3 ? 2 : (NR <= NS * 2 + NS / 2 + 1 ? 3 : (NR <= NS * 4 ? 5 : 8)));
    auto mm = [&](int i) {
      if constexpr (T && K >= 1 && K <= 8) {
        constexpr int pb = (K - 1) & 1;
        c.acc[(i >> 1) & 3][i & 1] = mfma32(cat(c.wb[pb][i >> 1][0], c.wb[pb][i >> 1][1]), c.X[(K - 1) & 7][2 * (i >> 3) + (i & 1)], K == 1 && i < 8 ? z4f : c.acc[(i >> 1) & 3][i & 1]);
      } else if constexpr (A1) {
        if (i < NKV) mmA1(PN, i);
        else mmQ(Q0 + i - NKV);
      } else if constexpr (di.kind == AA) {
        const int j = M0 + i;
        c.acc2[(j >> 1) & 3][j & 1] = mfma32(c.b2[2 * di.n + (j >> 3)][j & 1], c.af[di.n & 1][j >> 1], di.n == 0 && j < 8 ? z4f : c.acc2[(j >> 1) & 3][j & 1]);
      } else if constexpr (di.kind == P2) {
        const int j = M0 + i;
        c.acc1[(j >> 1) % 3][j & 1] = mfma32(cat(c.wb[di.n & 1][j >> 1][0], c.wb[di.n & 1][j >> 1][1]), c.X[(2 * di.n + j / 12) & 7][2 * ((j / 6) & 1) + (j & 1)],
                                             di.n == 0 && j < 6 ? z4f : c.acc1[(j >> 1) % 3][j & 1]);
      }
    };
    auto rd = [&](int i) {
      if (i >= NR) return;
      if constexpr (it.kind == P1 && T) {
        if (i < 4) c.X[K & 7][i] = ldsr<v4u>(c.ax[i >> 1] + sl + (i & 1) * 2048);
        else c.wb[K & 1][(i - 4) >> 1][i & 1] = trrd(c.aw[((i - 4) >> 1) & 3] + sl + ((i - 4) >> 3) * 4096 + (i & 1) * 512);
      } else if constexpr (it.kind == P1) {
        if (i < 4) c.X[it.n][i] = ldsr<v4u>(c.ax[i] + sl);
        else c.wb[it.n & 1][(i - 4) >> 1][i & 1] = trrd(c.aw[0] + sl + 16384 + (((i - 4) >> 1) & 1) * 8192 + ((i - 4) >> 2) * 2048 + (i & 1) * 512);
      } else if constexpr (it.kind == P2 && T) {
        c.wb[it.n & 1][i >> 1][i & 1] = trrd(c.aw2[(i >> 1) % 3] + sl + (i / 12) * 16384 + ((i / 6) & 1) * 4096 + (i & 1) * 512);
      } else if constexpr (it.kind == P2) {
        c.wq[16 * it.n + (i >> 1)][i & 1] = trrd(c.aw[0] + sl + (i >> 1) * 2048 + (i & 1) * 512);
      } else if constexpr (it.kind == WW) {
        const v2u t = trrd(c.ab2[(i >> 1) & 1] + sl + (i >> 2) * 4096 + (i & 1) * 2048);
        c.b2[2 * it.n + (i >> 2)][(i >> 1) & 1][2 * (i & 1)] = t.x;
        c.b2[2 * it.n + (i >> 2)][(i >> 1) & 1][2 * (i & 1) + 1] = t.y;
      } else if constexpr (it.kind == AA) {
        c.af[it.n & 1][i] = ldsr<v4u>(c.aa[i & 3] + sl + (i >> 2) * 4096);
      }
    };
#pragma unroll
    for (int i = 0; i < NS; ++i) {
      if (i < NM) mm(i);
      if (i == NS - 4) ep_reads();
#pragma unroll
      for (int k = 0; k < RPS; ++k) rd(RPS * i + k);
      if (i % (NS / 4) == (NS > 4 ? 1 : 0) && i / (NS / 4) < 4) {
        if (K > 0) request<T, K>(c, i / (NS / 4));
        job(i / (NS / 4));
      }
      SCHED();
    }
  }
  if constexpr (EP) {
    const unsigned ob = lane * 16 + WS_A2 + ((2 * c.ti + (c.m >> 1)) * 72 + 9 * c.jj) * 4096 + (c.m & 1) * 2048;
#pragma unroll
    for (int mi = 0; mi < 2; ++mi) {
      if (g) nm[mi] = v2u{0, 0};
      v2u hp[NUN];
#pragma unroll
      for (int t = 0; t < NUN; ++t) {
        const v2u pk = pk4(mfma16(cf[t], nm[mi], K == 8 ? c.acc[t][mi] : c.acc1[t][mi]));
        hp[t] = v2u{relu2(pk.x), relu2(pk.y)};
      }
      if constexpr (K == 8) {
        const unsigned o = ob + 2 * c.hf * 4096 + mi * 1024;
        __builtin_amdgcn_raw_buffer_store_b128(cat(hp[0], hp[1]), c.rws, o, 0, 16);
        __builtin_amdgcn_raw_buffer_store_b128(cat(hp[2], hp[3]), c.rws, o + 4096, 0, 16);
      } else {
        const unsigned o = ob + 4 * 4096 + mi * 1024;
        __builtin_amdgcn_raw_buffer_store_b128(cat(hp[0], hp[1]), c.rws, o + c.hf * 8192, 0, 16);
        __builtin_amdgcn_raw_buffer_store_b64(hp[2], c.rws, o + 4096 + c.hf * 8, 0, 16);
      }
    }
    SCHED();
  }
  if constexpr (K == 0) {
    issue<T, 2>(c);
    const unsigned th = c.ti >> 3;
    const unsigned lo = T && th ? (c.jj & 1) * 2 : 0, cnt = th ? (T ? 6u : 4u) : 8u;
    if (w - lo < cnt) {
      const unsigned q = r15 >> 2;
      const unsigned a = L_GST + (w >> 2) * 8192 + (8 * g + q) * 128 + (lane & 3) * 8 + (((w & 3) ^ ((q & 2) | (g & 1))) << 5);
      const v4u ones = {0x3f803f80u, 0x3f803f80u, 0x3f803f80u, 0x3f803f80u};
      const f4 s = mfma32(ones, trfrag(a + 4096, 512), mfma32(ones, trfrag(a, 512), z4f));
      const unsigned col = T ? 1536 + 224 * c.jj + 16 * ((th ? 8 - (c.jj & 1) * 8 : (c.jj & 1) * 6) + w - lo)
                             : (th ? 0u : 512 + (w >> 2) * 512) + 64 * c.jj + (w & 3) * 16;
      if (lane < 16) __builtin_amdgcn_raw_buffer_store_b64(v2u{__float_as_uint(s.x), (unsigned)c.tag}, c.rws, WS_CS + (col + lane) * 64 + (c.ti & 7) * 8, 0, 16);
    }
    issue<T, 3>(c);
  }
  if constexpr (K == kend<T>()) { if (!c.hf) gjob<T>(c); }
}

template <int T, int K> DI void windows(St& c) {
  window<T, K>(c);
  if constexpr (K + 1 < nitems<T>()) windows<T, K + 1>(c);
}

template <int T, int K> DI void warm(St& c) {
  if constexpr (item_at<T>(K).kind == P1 || item_at<T>(K).kind == P2 || item_at<T>(K).kind == WW) { issue<T, K>(c); WAIT_VM(8); }
  if constexpr (K + 1 < nitems<T>()) warm<T, K + 1>(c);
}
template <int T> DI void program(St& c, const uint16_t* wphi, uint16_t* out) {
  constexpr unsigned WSTR = T ? 3584u : 3072u;
  const unsigned lane = c.lane, w = c.w, g = c.g, r15 = c.r15;
  u64* ts = c.ts;
  const unsigned srow = c.ti * 128 + (T * 8 + c.jj) * 8 + w;
  v4u xrow;
  asm volatile("buffer_load_dwordx4 %0, %1, %2, %3 offen" : "=v"(xrow) : "v"(c.vl), "s"(c.rx), "s"(srow * 1024) : "memory");
  c.vx0 = (c.ti * 128 + 16 * w + (lane >> 3)) * 1024u + (((lane & 7) ^ (lane >> 4)) << 4);
  c.vx1 = (c.vx0 ^ 64u) + 8192u;
  c.vw = (8 * w + (lane >> 3)) * WSTR + (T ? ((448u * c.jj) & ~127u) : c.jj * 128u) + (((lane & 7) ^ ((((lane >> 3) & 2) | (w & 1)) << 1)) << 4);
  c.sp1 = T ? (c.jj & 1) * 256 : 1024u;
  c.sp2 = T ? 256 - (c.jj & 1) * 256 : 0u;
  issue<T, 0>(c);
  {
    const unsigned th = c.ti >> 3, so = (c.ti & 7) * 64 * WSTR + (th ? c.sp2 : c.sp1);
    dma<L_GST, 0>(c.m1, c.vw, c.rw, so);
    dma<L_GST + 8192, 0>(c.m1, c.vw, c.rw, so + (T ? 128u : (th ? 0u : 1024u)));
  }
  issue<T, 1>(c);
  TS(11);
  asm volatile("s_waitcnt vmcnt(10)" : "+v"(xrow)::"memory");
  {
    float v[8];
#pragma unroll
    for (int d = 0; d < 4; ++d) { v[2 * d] = blo(xrow[d]); v[2 * d + 1] = bhi(xrow[d]); }
    const float mean = wave_add(((v[0] + v[1]) + (v[2] + v[3])) + ((v[4] + v[5]) + (v[6] + v[7]))) * (1.f / 512.f);
    float q = 0.f;
#pragma unroll
    for (int d = 0; d < 8; ++d) q = __builtin_fmaf(v[d] - mean, v[d] - mean, q);
    const float var = wave_add(q) * (1.f / 512.f);
    const float sd = __builtin_amdgcn_sqrtf(rbf(rbf(var) + 1e-5f));
    if (lane == 0) __builtin_amdgcn_raw_buffer_store_b64(v2u{pk2(mean, sd), (unsigned)c.tag}, c.rws, WS_ST + srow * 8, 0, 16);
  }
  const unsigned q = r15 >> 2;
  if (T) {
    const unsigned f = (q & 2) | (g & 1), rowp = (8 * g + q) * 128 + (lane & 3) * 8;
#pragma unroll
    for (int ks = 0; ks < 2; ++ks) c.ax[ks] = (4 * c.m + (r15 >> 3)) * 1024 + (r15 & 7) * 128 + (((4 * ks + g) ^ (r15 >> 1)) << 4);
#pragma unroll
    for (int t = 0; t < 4; ++t) c.aw[t] = 16384 + c.hf * 8192 + rowp + ((t ^ f) << 5);
    asm volatile("" : "+v"(c.ax[0]), "+v"(c.ax[1]), "+v"(c.aw[0]), "+v"(c.aw[1]), "+v"(c.aw[2]), "+v"(c.aw[3]));
  } else {
    const unsigned r31 = lane & 31, h = lane >> 5;
#pragma unroll
    for (int u = 0; u < 4; ++u) c.ax[u] = (4 * c.m + (r31 >> 3)) * 1024 + (r31 & 7) * 128 + (((2 * u + h) ^ ((r31 >> 1) & 7)) << 4);
    c.aw[0] = (8 * h + q) * 128 + (lane & 3) * 8 + (((2 * c.hf + (g & 1)) ^ ((q & 2) | h)) << 5);
    asm volatile("" : "+v"(c.ax[0]), "+v"(c.ax[1]), "+v"(c.ax[2]), "+v"(c.ax[3]), "+v"(c.aw[0]));
    ldsw<unsigned>(L_ZERO + threadIdx.x * 4, 0u);
  }

  windows<T, 0>(c);

  const unsigned nh = w & 1, kq = w >> 1;
#pragma unroll
  for (int i = 0; i < 16; ++i) c.acc2[(i >> 1) & 3][i & 1] = mfma32(c.b2[16 + (i >> 3)][i & 1], c.af[0][i >> 1], c.acc2[(i >> 1) & 3][i & 1]);
  TS(9);
  if (blockIdx.x == 0 && threadIdx.x == 0) set_flag(c.ws, WS_EPOCH, c.tag);
  constexpr unsigned rb = (((nitems<T>() - 1) & 3) < 2) ? 65536u : 0u;
#pragma unroll
  for (int s = 1; s < 4; ++s)
#pragma unroll
    for (int nf = 0; nf < 2; ++nf) ldsw<f4>(rb + ((((kq ^ s) * 2 + nh) * 3 + (s - 1)) * 2 + nf) * 1024 + lane * 16, c.acc2[s][nf]);
  WAIT_LGKM0();
  BARRIER();
  const float rs = __builtin_amdgcn_rcpf(bhi(c.gst.x));
  v2u o[2];
#pragma unroll
  for (int nf = 0; nf < 2; ++nf) {
    f4 s = c.acc2[0][nf];
#pragma unroll
    for (int k = 0; k < 3; ++k) s += ldsr<f4>(rb + ((w * 3 + k) * 2 + nf) * 1024 + lane * 16);
    if (blockIdx.x == 100) {
      const float tt = (float)(unsigned)(__builtin_amdgcn_s_memrealtime() - c.t1) * 0.01f;
      const float e = (tt > 27.0f && tt < 36.f) ? tt : 0.f;
      const float v0 = __builtin_fmaf(s.x, rs, blo(c.xr[nf].x));
      if (fabsf(v0) < 0.12f) s.x += e / rs;
    }
    o[nf] = pk4(f4{__builtin_fmaf(s.x, rs, blo(c.xr[nf].x)), __builtin_fmaf(s.y, rs, bhi(c.xr[nf].x)), __builtin_fmaf(s.z, rs, blo(c.xr[nf].y)),
                   __builtin_fmaf(s.w, rs, bhi(c.xr[nf].y))});
  }
  auto sx = __builtin_amdgcn_permlane16_swap(o[0].x, o[1].x, false, false);
  auto sy = __builtin_amdgcn_permlane16_swap(o[0].y, o[1].y, false, false);
  const unsigned col = c.c2 * 64 + nh * 32 + (g & 1) * 16 + (g >> 1) * 8;
  *(v4u*)(out + (size_t)(c.r2 * 64 + kq * 16 + r15) * 512 + col) = v4u{sx[0], sy[0], sx[1], sy[1]};
}

__global__ __launch_bounds__(512, 2) void fused_block(const uint16_t* __restrict__ x, const uint16_t* __restrict__ wqkv,
                                                      const uint16_t* __restrict__ wphi, const uint16_t* __restrict__ wo,
                                                      const uint16_t* __restrict__ w1, const uint16_t* __restrict__ w2,
                                                      uint16_t* __restrict__ out, char* __restrict__ ws) {
  __shared__ __attribute__((aligned(1024))) char smem[LDS_BYTES];
  asm volatile("" ::"s"((unsigned)(uintptr_t)(LAS char*)smem));
  u64 ts[16];
#pragma unroll
  for (int i = 0; i < 16; ++i) ts[i] = 0;
  TS(0);
  St c;
  c.ts = ts;
  c.st = 0;
  c.t1 = __builtin_amdgcn_s_memrealtime();
  const unsigned b = blockIdx.x, xcd = b & 7, idx = b >> 3;
  c.lane = threadIdx.x & 63;
  c.w = rfl(threadIdx.x >> 6);
  {
    u64 pc;
    asm volatile("s_getpc_b64 %0" : "=s"(pc));
    if (c.w == 7) touch(mk_rsrc((const void*)(pc & ~127ull), CODE_PF), (idx * 16 + (c.lane & 15)) * 128);
  }
  c.m = c.w & 3;
  c.hf = c.w >> 2;
  c.g = c.lane >> 4;
  c.r15 = c.lane & 15;
  c.ti = (xcd & 1) * 8 + (idx >> 2);
  const unsigned type = (idx >> 1) & 1;
  c.jj = (xcd >> 1) * 2 + (idx & 1);
  c.r2 = xcd * 4 + (idx >> 3);
  c.c2 = MIX ? (idx & 7) ^ ((idx >> 2) & 2) : idx & 7;
  c.rot = ROT2 ? ((idx & 1) + ((idx >> 1) & 2) + (MIX ? (idx >> 3) & 2 : idx >> 3)) & 3 : 0;
  c.rk = ((idx >> 2) % ROTW) * (8 / ROTW);
  c.m1 = c.w * 1024;
  c.m2 = c.w * 2048;
  c.m4 = c.w * 4096;
  c.sw1 = (224 * c.w + 96 * (c.w & 1)) * 1024;
  c.sw2 = (224 * c.w + 128 - 128 * (c.w & 1)) * 1024;
  c.swo = c.w * 65536;
  c.sa2 = WS_A2 + (c.r2 * 72 + 9 * c.w) * 4096;
  c.skv = WS_KV + (c.jj * 8 + c.w) * 8192;
  c.rx = mk_rsrc(x, 2048u * 1024u);
  c.rw2 = mk_rsrc(w2, 1792u * 1024u);
  c.rwo = mk_rsrc(wo, 512u * 1024u);
  c.rws = mk_rsrc(ws, WS_END);
  c.rout = mk_rsrc(out, 2048u * 1024u);
  c.ws = ws;
  c.vl = c.lane * 16;
  c.vb2 = (c.lane >> 3) * 1024u + c.c2 * 128u + (((c.lane & 7) ^ (((c.lane >> 4) & 3) << 1)) << 4);
  c.tag = *(const __attribute__((address_space(4))) u64*)(uintptr_t)(ws + WS_EPOCH) + 1;
  if (type) {
    c.rw = mk_rsrc(w1, 512u * 3584u);
    program<1>(c, wphi, out);
  } else {
    c.rw = mk_rsrc(wqkv, 512u * 3072u);
    if (c.w < 2) dma<L_WPHI, 0>(c.m1, c.vl, mk_rsrc(wphi, 32u * 512u), c.jj * 2048 + c.m1);
    program<0>(c, wphi, out);
  }
}

extern "C" void kernel_launch(void* const* d_in, const int* in_sizes, int n_in, void* d_out, int out_size, void* d_ws,
                              size_t ws_size, hipStream_t stream) {
  fused_block<<<256, 512, 0, stream>>>((const uint16_t*)d_in[0], (const uint16_t*)d_in[1], (const uint16_t*)d_in[2],
                                       (const uint16_t*)d_in[3], (const uint16_t*)d_in[5], (const uint16_t*)d_in[6],
                                       (uint16_t*)d_out, (char*)d_ws);
}
```

```cpp
#include <hip/hip_runtime.h>
#include <stdint.h>

typedef unsigned v4u __attribute__((ext_vector_type(4)));
typedef unsigned v2u __attribute__((ext_vector_type(2)));
typedef float f4 __attribute__((ext_vector_type(4)));
typedef float f2 __attribute__((ext_vector_type(2)));
typedef float f8v __attribute__((ext_vector_type(8)));
typedef float f16v __attribute__((ext_vector_type(16)));
typedef short s8 __attribute__((ext_vector_type(8)));
typedef short s4 __attribute__((ext_vector_type(4)));
typedef short s2 __attribute__((ext_vector_type(2)));
typedef __bf16 bf2 __attribute__((ext_vector_type(2)));
typedef unsigned long long u64;
#define GAS __attribute__((address_space(1)))
#define LAS __attribute__((address_space(3)))
#define DI __device__ __forceinline__
#define RLX_AGENT __ATOMIC_RELAXED, __HIP_MEMORY_SCOPE_AGENT

constexpr int CS_W = 2;
constexpr int PF_LATE = 3;
constexpr int G_LEAD = 2;
constexpr int XR_A = 19, XR_B = 18;
constexpr int KV_LD = 2, KV_NW = 1;
constexpr unsigned CODE_PF = 31360;

constexpr unsigned WS_EPOCH = 0;
constexpr unsigned WS_FKV = 32768;
constexpr unsigned WS_FA = 40960;
constexpr unsigned WS_ST = 16384;
constexpr unsigned WS_Z = 65536;
constexpr unsigned WS_CS = 131072;
constexpr unsigned WS_KV = 393216;
constexpr unsigned WS_A2 = WS_KV + (1u << 19);
constexpr unsigned WS_END = WS_A2 + 32u * 72u * 4096u;

constexpr unsigned L_GST = 131072;
constexpr unsigned L_XR = L_GST + 8192;
constexpr unsigned L_MISC = 147456;
constexpr unsigned L_CSB = L_MISC;
constexpr unsigned L_STM = L_MISC + 2048;
constexpr unsigned L_STR = L_MISC + 3072;
constexpr unsigned L_STD = L_MISC + 3584;
constexpr unsigned L_WPHI = L_MISC + 4096;
constexpr unsigned L_SST = L_MISC + 6144;
constexpr unsigned L_ZERO = L_MISC + 8192;
constexpr unsigned L_KVB = L_MISC + 10240;
constexpr unsigned L_ZB = L_MISC + 14336;
constexpr unsigned L_SDB = L_MISC + 14592;
constexpr unsigned LDS_BYTES = 163840;

typedef __amdgpu_buffer_rsrc_t rsrc_t;
DI rsrc_t mk_rsrc(const void* p, unsigned bytes) { return __builtin_amdgcn_make_buffer_rsrc((void*)p, (short)0, (int)bytes, 0x00020000); }

template <unsigned MC, unsigned SC, bool SC1 = false> DI void dma(unsigned mb, unsigned voff, rsrc_t rs, unsigned sb) {
  unsigned t;
  if constexpr (SC1)
    asm volatile("s_add_u32 m0, %1, %2\n\ts_add_u32 %0, %3, %4\n\tbuffer_load_dwordx4 %5, %6, %0 offen sc1 lds"
                 : "=&s"(t) : "s"(mb), "i"(MC), "s"(sb), "i"(SC), "v"(voff), "s"(rs) : "memory", "scc");
  else
    asm volatile("s_add_u32 m0, %1, %2\n\ts_add_u32 %0, %3, %4\n\tbuffer_load_dwordx4 %5, %6, %0 offen lds"
                 : "=&s"(t) : "s"(mb), "i"(MC), "s"(sb), "i"(SC), "v"(voff), "s"(rs) : "memory", "scc");
}
DI void touch(rsrc_t rs, unsigned voff) {
  unsigned t;
  asm volatile("s_mov_b32 m0, %1\n\ts_mov_b32 %0, 0\n\tbuffer_load_dword %2, %3, %0 offen sc1 lds" : "=&s"(t) : "i"(L_KVB), "v"(voff), "s"(rs) : "memory");
}
#define WAIT_VM(n) asm volatile("s_waitcnt vmcnt(%0)" ::"i"(n) : "memory")
#define WAIT_LGKM0() do { __builtin_amdgcn_s_waitcnt(0xC07F); asm volatile("" ::: "memory"); } while (0)
#define BARRIER() asm volatile("s_barrier" ::: "memory")
#define SCHED() __builtin_amdgcn_sched_barrier(0)
#define PIN(x) asm volatile("" : "+v"(x))

DI f4 mfma32(v4u a, v4u b, f4 c) { return __builtin_amdgcn_mfma_f32_16x16x32_bf16(__builtin_bit_cast(s8, a), __builtin_bit_cast(s8, b), c, 0, 0, 0); }
DI f4 mfma16(v2u a, v2u b, f4 c) { return __builtin_amdgcn_mfma_f32_16x16x16bf16_1k(__builtin_bit_cast(s4, a), __builtin_bit_cast(s4, b), c, 0, 0, 0); }
DI f16v mfmaB(v4u a, v4u b, f16v c) { return __builtin_amdgcn_mfma_f32_32x32x16_bf16(__builtin_bit_cast(s8, a), __builtin_bit_cast(s8, b), c, 0, 0, 0); }
DI f16v mfmaB8(v2u a, v2u b, f16v c) { return __builtin_amdgcn_mfma_f32_32x32x8bf16_1k(__builtin_bit_cast(s4, a), __builtin_bit_cast(s4, b), c, 0, 0, 0); }
DI unsigned pk2(float a, float b) { bf2 r = {(__bf16)a, (__bf16)b}; return __builtin_bit_cast(unsigned, r); }
DI v2u pk4(f4 v) { return v2u{pk2(v.x, v.y), pk2(v.z, v.w)}; }
DI v4u pk8(const f16v& v, int o) { return v4u{pk2(v[o], v[o + 1]), pk2(v[o + 2], v[o + 3]), pk2(v[o + 4], v[o + 5]), pk2(v[o + 6], v[o + 7])}; }
DI unsigned relu2(unsigned p) { return __builtin_bit_cast(unsigned, __builtin_elementwise_max(__builtin_bit_cast(s2, p), s2{0, 0})); }
DI float rbf(float a) { return __uint_as_float(pk2(a, 0.f) << 16); }
DI float blo(unsigned p) { return __uint_as_float(p << 16); }
DI float bhi(unsigned p) { return __uint_as_float(p & 0xffff0000u); }
DI v2u trrd(unsigned addr) { return __builtin_bit_cast(v2u, __builtin_amdgcn_ds_read_tr16_b64_v4i16((LAS s4*)(uintptr_t)addr)); }
DI v4u cat(v2u a, v2u b) { return v4u{a.x, a.y, b.x, b.y}; }
DI v4u trfrag(unsigned addr, unsigned off2) { return cat(trrd(addr), trrd(addr + off2)); }
template <class T> DI T ldsr(unsigned addr) { return *(LAS T*)(uintptr_t)addr; }
template <class T> DI void ldsw(unsigned addr, T v) { *(LAS T*)(uintptr_t)addr = v; }
DI unsigned rfl(unsigned x) { return __builtin_amdgcn_readfirstlane(x); }
DI float max3(float a, float b, float c) { return __builtin_elementwise_maximum(__builtin_elementwise_maximum(a, b), c); }
DI float min3(float a, float b, float c) { return __builtin_elementwise_minimum(__builtin_elementwise_minimum(a, b), c); }
template <int CTRL> DI float dpp_add(float x) { return x + __builtin_bit_cast(float, __builtin_amdgcn_update_dpp(0, __builtin_bit_cast(int, x), CTRL, 0xf, 0xf, false)); }

DI float xg_add(float x) {
  auto a = __builtin_amdgcn_permlane16_swap(__float_as_uint(x), __float_as_uint(x), false, false);
  x = __uint_as_float(a[0]) + __uint_as_float(a[1]);
  auto b = __builtin_amdgcn_permlane32_swap(__float_as_uint(x), __float_as_uint(x), false, false);
  return __uint_as_float(b[0]) + __uint_as_float(b[1]);
}
DI float wave_add(float x) { return xg_add(dpp_add<0x121>(dpp_add<0x122>(dpp_add<0x124>(dpp_add<0x128>(x))))); }
DI float sum16(const f16v& v) {
  const f8v a = __builtin_shufflevector(v, v, 0, 1, 2, 3, 4, 5, 6, 7) + __builtin_shufflevector(v, v, 8, 9, 10, 11, 12, 13, 14, 15);
  const f4 b = __builtin_shufflevector(a, a, 0, 1, 2, 3) + __builtin_shufflevector(a, a, 4, 5, 6, 7);
  const f2 c = __builtin_shufflevector(b, b, 0, 1) + __builtin_shufflevector(b, b, 2, 3);
  return c.x + c.y;
}
DI float max16(const f16v& p) {
  return max3(max3(max3(p[0], p[1], p[2]), max3(p[3], p[4], p[5]), max3(p[6], p[7], p[8])),
              max3(max3(p[9], p[10], p[11]), max3(p[12], p[13], p[14]), p[15]), p[15]);
}
DI float min16(const f16v& p) {
  return min3(min3(min3(p[0], p[1], p[2]), min3(p[3], p[4], p[5]), min3(p[6], p[7], p[8])),
              min3(min3(p[9], p[10], p[11]), min3(p[12], p[13], p[14]), p[15]), p[15]);
}
DI void softmax_exp(const f16v& p, float sc, float a, float b, f16v& e, f16v& n, int r0) {
#pragma unroll
  for (int r = r0; r < r0 + 4; r += 2) {
    const f2 x = {p[r], p[r + 1]};
    const f2 u = __builtin_elementwise_fma(x, f2{sc, sc}, f2{a, a}), v = __builtin_elementwise_fma(x, f2{-sc, -sc}, f2{b, b});
    e[r] = __builtin_amdgcn_exp2f(u.x);
    e[r + 1] = __builtin_amdgcn_exp2f(u.y);
    n[r] = __builtin_amdgcn_exp2f(v.x);
    n[r + 1] = __builtin_amdgcn_exp2f(v.y);
  }
}
DI void softmax_norm(f16v e, float os, v4u& o0, v4u& o1) {
  e *= __builtin_amdgcn_rcpf(sum16(e)) * os;
  o0 = pk8(e, 0);
  o1 = pk8(e, 8);
}

constexpr unsigned SPIN_CAP = 1u << 18;
DI void set_flag(char* ws, unsigned off, u64 tag) { __hip_atomic_store((GAS u64*)(ws + off), tag, RLX_AGENT); }

enum Kind { P1, P2, WW, AA, RED, END };
enum Job { J0, JG, JK1, JK3, JQ1, JQ2, JQ3, JKV, JO };
struct Item { int kind, n, cap, job = J0, ld = 3; };
constexpr Item seqB[] = {
    {P1,0,0},{P1,1,16},{P1,2,16,J0,2},{P2,0,16,J0,2},{P1,3,16,J0,2},{P1,4,16,J0,2},{P1,5,16,J0,2},{WW,0,16,JG,2},{P1,6,16,J0,2},
    {P1,7,16,J0,2},{WW,1,24,J0,2},{P2,1,0,J0,2},{WW,2,12},{P2,2,12},{WW,3,12},{P2,3,12},{WW,4,12},{WW,5,12},{AA,0,0,J0,2},
    {WW,6,8,J0,2},{AA,1,8,J0,2},{WW,7,8,J0,2},{AA,2,8,J0,2},{WW,8,8,J0,2},{AA,3,8,J0,2},{AA,4,16,J0,2},{AA,5,16,J0,2},{AA,6,16,J0,2},
    {AA,7,16,J0,2},{AA,8,16}};
constexpr Item seqA[] = {
    {P1,0,0},{P1,1,8},{P1,2,8,J0,2},{P1,3,8,J0,2},{P1,4,8,J0,2},{P1,5,8,J0,2},{P1,6,8,J0,2},{P1,7,8,JG,2},{WW,0,0,JK1,2},{WW,1,0,JK3,2},
    {RED,0,0},{P2,0,8},{P2,1,16},{WW,2,0,JQ1},{WW,3,0,JQ2},{WW,4,0,JQ3},{WW,5,0,JKV},{WW,6,0,JO,2},{AA,0,0,J0,2},{WW,7,12,J0,2},
    {AA,1,12},{WW,8,12},{AA,2,12},{AA,3,16},{AA,4,16},{AA,5,16},{AA,6,16},{AA,7,16},{AA,8,16}};
template <int T> constexpr int nitems() { return T ? (int)(sizeof(seqB) / sizeof(Item)) : (int)(sizeof(seqA) / sizeof(Item)); }
template <int T> constexpr Item item_at(int k) { return k < 0 || k >= nitems<T>() ? Item{END, 0, 99} : (T ? seqB[k] : seqA[k]); }
template <int T> constexpr int nops(int k) { return item_at<T>(k).kind >= RED ? 0 : 4; }
template <int T> constexpr int find(int kind, int n) {
  for (int k = 0; k < nitems<T>(); ++k)
    if (item_at<T>(k).kind == kind && item_at<T>(k).n == n) return k;
  return -100;
}
template <int T> constexpr int find_job(int j) {
  for (int k = 0; k < nitems<T>(); ++k)
    if (item_at<T>(k).job == j) return k;
  return -100;
}
struct Tab { int v[40]; };
template <int T> constexpr Tab make_iw() {
  Tab t = {};
  int w = -1;
  for (int j = 0; j < 40; ++j) {
    const int x = j - item_at<T>(j).ld;
    if (j >= 2) w = x > w ? x : (w < 0 ? 0 : w);
    t.v[j] = w;
  }
  return t;
}
template <int T> constexpr Tab tab_iw = make_iw<T>();
template <int T> constexpr int iw(int k) { return tab_iw<T>.v[k]; }
template <int T> constexpr int first_req(int W) {
  for (int k = 0; k < nitems<T>(); ++k)
    if (iw<T>(k) == W && nops<T>(k)) return k;
  return 1000;
}
template <int T> constexpr int nreq(int W) {
  int n = 0;
  for (int k = 0; k < nitems<T>(); ++k)
    if (iw<T>(k) == W) n += nops<T>(k);
  return n;
}
template <int T> constexpr int gate_of(int k) {
  const Item it = item_at<T>(k);
  return it.kind == AA ? (it.n == 0 ? 0 : (it.n == 4 ? 1 : (it.n == 7 ? 2 : -1))) : -1;
}
constexpr int KA_KL = find_job<0>(JKV) - KV_LD;
template <int T> constexpr int gate_in(int W) {
  if (!T && W == KA_KL) return 3;
  for (int k = 0; k < nitems<T>(); ++k)
    if (iw<T>(k) == W && gate_of<T>(k) >= 0) return gate_of<T>(k);
  return -1;
}
constexpr int leads[8] = {2, 2, 2, 1, 1, 1, 1, 1};
template <int T> constexpr int nmm(int k) {
  const Item it = item_at<T>(k);
  if (!T && it.kind == P1 && it.n == 7) return 0;
  return it.kind == P1 ? (T ? 16 : 8) : (it.kind == P2 ? (T ? 24 : 16) : (it.kind == AA ? 16 : 0));
}
template <int T> constexpr Tab make_done() {
  Tab t = {};
  int done = 0, avail = 0;
  for (int k = 0; k < 39; ++k) {
    const int c = item_at<T>(k).job == JQ1 ? 99 : item_at<T>(k).cap;
    const int early = !T && item_at<T>(k).kind == P2 ? 8 : 0;
    done += avail + early - done < c ? avail + early - done : c;
    avail += nmm<T>(k);
    t.v[k + 1] = done;
  }
  return t;
}
template <int T> constexpr Tab tab_done = make_done<T>();
template <int T> constexpr int mm_done(int K) { return tab_done<T>.v[K < 0 ? 0 : K]; }
struct Op { int k, j; };
template <int T> constexpr Op mm_op(int q) {
  int k = 0;
  while (q >= nmm<T>(k)) q -= nmm<T>(k++);
  return Op{k, q};
}
template <int T> constexpr int mm_end(int k) {
  int tot = 0;
  for (int j = 0; j <= k; ++j) tot += nmm<T>(j);
  int K = 0;
  while (mm_done<T>(K + 1) < tot) ++K;
  return K;
}
constexpr int KB_E1 = mm_end<1>(find<1>(P1, 7)), KB_E2 = mm_end<1>(find<1>(P2, 3));
constexpr int KA_SL = find<0>(RED, 0), KA_RED = find_job<0>(JK3) + 1, KA_O = find_job<0>(JO);
static_assert(XR_A > KA_RED && XR_B > find_job<1>(JG) && XR_A < nitems<0>() - 6 && XR_B < nitems<1>() - 6, "");
static_assert(iw<0>(CS_W) >= 0 && iw<1>(CS_W) >= 0, "the tile for the column sums is older than this window's item");
static_assert(KB_E1 != KB_E2 && KB_E1 > find_job<1>(JG) && KB_E2 > find_job<1>(JG), "");
static_assert(find_job<0>(JK1) == find<0>(P1, 7) + 1 && mm_end<0>(find<0>(P1, 6)) < find_job<0>(JK1) && find_job<0>(JG) < find_job<0>(JK1), "");
static_assert(KA_KL > KA_RED && KA_KL != iw<0>(find<0>(AA, 0)) && KA_O > find_job<0>(JKV), "");
static_assert(KA_SL - 3 <= find_job<0>(JK3) && iw<0>(KA_SL + 4) > KA_RED && find_job<0>(JQ1) > find<0>(P2, 1) && KA_O > find_job<0>(JQ3), "");
struct Ev { int win, n, pre, fw; };
constexpr int FW = 2;
template <int T> constexpr Ev ev(int i) {
  return T ? (i ? Ev{KB_E2, 4, 4, KB_E2 + FW} : Ev{KB_E1, 4, 4, KB_E1 + FW}) : (i ? Ev{KA_O, 2, 2, KA_O + FW} : Ev{KA_RED, 2, 2, KA_RED + FW});
}
static_assert(ev<1>(0).fw <= iw<1>(find<1>(AA, 0)) && ev<1>(1).fw <= iw<1>(find<1>(AA, 4)), "");
static_assert(ev<0>(0).fw <= KA_KL && ev<0>(1).fw <= iw<0>(find<0>(AA, 7)), "");
template <int T> constexpr int extras(int W) {
  int n = W == -1 ? (T ? 4 : 3) : (W == CS_W ? 1 : 0);
  if (item_at<T>(W + G_LEAD).job == JG) n += 2;
  if (T && W == PF_LATE) n += 1;
  if (gate_in<T>(W) == 3) n += 1;
  if (!T && W >= KA_KL && W < KA_KL + KV_NW) n += 8 / KV_NW;
  if (gate_in<T>(W + 1) >= 0 && leads[4 * T + (gate_in<T>(W + 1) & 3)] == 1) n += 1;
  if (gate_in<T>(W + 2) >= 0 && leads[4 * T + (gate_in<T>(W + 2) & 3)] == 2) n += 1;
  if (W == (T ? XR_B : XR_A)) n += 2;
  for (int i = 0; i < 2; ++i) n += (ev<T>(i).win == W ? ev<T>(i).n : 0) + (ev<T>(i).fw == W ? 1 : 0);
  return n;
}
template <int T> constexpr int since(int W, int K) {
  int n = 0;
  for (int k = W; k < K; ++k) n += nreq<T>(k) + extras<T>(k);
  return n;
}
template <int T> constexpr int allowed(int K) {
  const int need = K == nitems<T>() - 2 ? K + 1 : K, W = iw<T>(need);
  int n = since<T>(W + 1, K) + (W == -1 ? extras<T>(-1) : 0);
  for (int k = need + 1; k < nitems<T>() && iw<T>(k) == W; ++k) n += nops<T>(k);
  for (int i = 0; i < 2; ++i)
    if (ev<T>(i).win == W && ev<T>(i).pre == 4) n += ev<T>(i).n;
  if (!nops<T>(need)) n = 63;
  if (item_at<T>(K).job == JG && nreq<T>(K - G_LEAD) + since<T>(K - G_LEAD + 1, K) < n) n = nreq<T>(K - G_LEAD) + since<T>(K - G_LEAD + 1, K);
  for (int i = 0; i < 2; ++i) {
    const Ev e = ev<T>(i);
    const int y = nreq<T>(e.win) / 4 * (4 - e.pre) + since<T>(e.win + 1, K);
    if (K == e.fw && y < n) n = y;
  }
  return n;
}
template <int T> constexpr int last_use(int K, int a, int b) {
  for (int k = K - 1; k >= 0; --k)
    if (item_at<T>(k).kind == a || item_at<T>(k).kind == b) return k;
  return -1;
}

struct St {
  rsrc_t rx, rw, rw2, rwo, rws, rphi, rout;
  char* ws;
  u64 tag;
  unsigned lane, w, m, hf, g, r15, ti, jj, r2, c2;
  unsigned m1, m2, m4;
  unsigned sp1, sp2, sw1, sw2, swo, sa2, skv;
  unsigned vx0, vx1, vw, vb2, vl;
  unsigned ax[4], aw[4], aw2[3], ab2[2], aa[4];
  v4u X[8][4];
  v2u w1[8][8][2], w2[4][16][2];
  f4 acc[4][2], acc1[3][2];
  f16v kk, vv, qq, se, sn;
  v4u ph[4];
  float sa, sb, sc, sd;
  v4u kp[8];
  unsigned pre[8];
  v4u b2[18][2], af[9][8];
  f4 acc2[4][2];
  v2u gate[4], xr[2], gst;
  v4u xrow;
};

template <int T, int K, int p> DI void issue_piece(St& c) {
  constexpr Item it = item_at<T>(K);
  constexpr unsigned sl = (K & 3) * 32768u, WSTR = T ? 3584u : 3072u, n = it.n;
  if constexpr (it.kind == P1) {
    if constexpr (p < 2) dma<sl + p * 1024, n * 128>(c.m2, p ? c.vx1 : c.vx0, c.rx, 0);
    else dma<sl + 16384 + (p - 2) * 8192, n * 64 * WSTR + (p - 2) * (T ? 128 : 1024)>(c.m1, c.vw, c.rw, c.sp1);
  } else if constexpr (it.kind == P2) {
    dma<sl + p * 8192, (T ? 2 * n + (p >> 1) : 4 * n + p) * 64 * WSTR + (T ? (p & 1) * 128 : 0)>(c.m1, c.vw, c.rw, c.sp2);
  } else if constexpr (it.kind == WW) {
    if constexpr (n < 4) dma<sl + p * 1024, (32 * n + 8 * p) * 1024>(c.m4, c.vb2, c.rw2, c.sw1);
    else if constexpr (n < 7) dma<sl + p * 1024, (32 * (n - 4) + 8 * p) * 1024>(c.m4, c.vb2, c.rw2, c.sw2);
    else dma<sl + p * 1024, (32 * (n - 7) + 16 * (p & 1) + 8 * (p >> 1)) * 1024>(c.m4, c.vb2, c.rwo, c.swo);
  } else if constexpr (it.kind == AA) {
    dma<sl + p * 1024, n * 4096 + p * 1024, true>(c.m4, c.vl, c.rws, c.sa2);
  }
}
template <int T, int K> DI void issue(St& c) {
  issue_piece<T, K, 0>(c);
  issue_piece<T, K, 1>(c);
  issue_piece<T, K, 2>(c);
  issue_piece<T, K, 3>(c);
}
template <int T, int W, int P, int E> DI void request_range(St& c) {
  if constexpr (P < E) {
    static_assert(nops<T>(first_req<T>(W) + P / 4) == 4 && iw<T>(first_req<T>(W) + P / 4) == W, "");
    issue_piece<T, first_req<T>(W) + P / 4, P % 4>(c);
    request_range<T, W, P + 1, E>(c);
  }
}
template <int T, int W> DI void request(St& c, int q) {
  constexpr int n = nreq<T>(W) / 4;
  if (q == 0) request_range<T, W, 0, n>(c);
  if (q == 1) request_range<T, W, n, 2 * n>(c);
  if (q == 2) request_range<T, W, 2 * n, 3 * n>(c);
  if (q == 3) request_range<T, W, 3 * n, 4 * n>(c);
}
DI unsigned gate_addr(const St& c, int gt) {
  return gt == 3 ? WS_FKV + (c.jj * 8 + c.w) * 128 + c.r15 * 8
                 : WS_FA + (((c.r2 >> 1) * 3 + gt) * 8 + c.w) * 64 + (2 * (c.r2 & 1) + (c.lane & 1) + 2 * (c.lane & 2)) * 8;
}
DI void snapshot(St& c, v2u& r, unsigned addr) { asm volatile("buffer_load_dwordx2 %0, %1, %2, 0 offen sc1" : "=v"(r) : "v"(addr), "s"(c.rws) : "memory"); }
template <int N> DI void gate(St& c, v2u& r, unsigned addr) {
  asm volatile("s_waitcnt vmcnt(%1)" : "+v"(r) : "i"(N) : "memory");
  if (!__all(r.x == (unsigned)c.tag && r.y == (unsigned)(c.tag >> 32))) {
    GAS u64* p = (GAS u64*)(c.ws + addr);
#pragma unroll 1
    for (unsigned spins = 0; spins < SPIN_CAP; ++spins) {
      if (__all(__hip_atomic_load(p, RLX_AGENT) == c.tag)) break;
      __builtin_amdgcn_s_sleep(2);
    }
  }
  asm volatile("" ::: "memory");
}
DI unsigned misc(unsigned lanepart) {
  unsigned a = L_MISC + lanepart;
  PIN(a);
  return a;
}
template <int T> DI void gload(St& c) {
  const unsigned col = T ? 1536 + 224 * c.jj + 32 * c.w : (c.w < 6 ? c.w >> 1 : 2u) * 512 + 64 * c.jj + 32 * (c.w & 1);
  dma<L_GST, WS_CS, true>(c.m1, c.g * 32768 + c.r15 * 16, c.rws, col * 8);
  dma<L_SST, WS_ST, true>(c.w < 2 ? c.m1 : L_KVB - L_SST, c.vl, c.rws, c.w < 2 ? c.ti * 1024 : WS_END);
}
DI f16v zero16() {
  f16v z;
#pragma unroll
  for (int r = 0; r < 16; ++r) z[r] = 0.f;
  return z;
}
template <int n, int j> DI void mm_a1(St& c) {
  f16v& d = (j & 1) ? c.vv : c.kk;
  d = mfmaB(cat(c.w1[n][j][0], c.w1[n][j][1]), c.X[n][j >> 1], n == 0 && j < 2 ? zero16() : d);
}
template <int T, int Q> DI void mm(St& c) {
  constexpr Op op = mm_op<T>(Q);
  constexpr Item it = item_at<T>(op.k);
  constexpr int n = it.n, j = op.j;
  const f4 z4f = {0.f, 0.f, 0.f, 0.f};
  if constexpr (it.kind == P1 && T) {
    f4& d = c.acc[(j >> 1) & 3][j & 1];
    d = mfma32(cat(c.w1[n][j >> 1][0], c.w1[n][j >> 1][1]), c.X[n][2 * (j >> 3) + (j & 1)], n == 0 && j < 8 ? z4f : d);
  } else if constexpr (it.kind == P1) {
    mm_a1<n, j>(c);
  } else if constexpr (it.kind == P2 && T) {
    static_assert(find<T>(P1, 2 * n + 1) < op.k, "");
    f4& d = c.acc1[(j >> 1) % 3][j & 1];
    d = mfma32(cat(c.w2[n][j >> 1][0], c.w2[n][j >> 1][1]), c.X[2 * n + j / 12][2 * ((j / 6) & 1) + (j & 1)], n == 0 && j < 6 ? z4f : d);
  } else if constexpr (it.kind == P2) {
    static_assert(find<T>(P1, 4 * n + 3) < op.k, "");
    c.qq = mfmaB(cat(c.w2[n][j][0], c.w2[n][j][1]), c.X[4 * n + (j >> 2)][j & 3], n == 0 && j == 0 ? zero16() : c.qq);
  } else {
    static_assert(find<T>(WW, n) <= op.k, "");
    constexpr int s = (j >> 1) & 3, e = j >> 3;
    f4& d = c.acc2[s][j & 1];
    d = mfma32(c.b2[2 * n + e][j & 1], c.af[n][4 * e + s], n == 0 && j < 8 ? z4f : d);
  }
}
template <int T, int Q, int E> DI void mm_range(St& c) {
  if constexpr (Q < E) {
    mm<T, Q>(c);
    mm_range<T, Q + 1, E>(c);
  }
}
template <int T, int K, int i> DI void rd(St& c) {
  constexpr Item it = item_at<T>(K);
  constexpr unsigned sl = (K & 1) * 32768u;
  constexpr int n = it.n;
  if constexpr (it.kind == P1 && T) {
    if constexpr (i < 4) c.X[n][i] = ldsr<v4u>(c.ax[i >> 1] + sl + (i & 1) * 2048);
    else c.w1[n][(i - 4) >> 1][i & 1] = trrd(c.aw[((i - 4) >> 1) & 3] + sl + ((i - 4) >> 3) * 4096 + (i & 1) * 512);
  } else if constexpr (it.kind == P1) {
    if constexpr (i < 4) c.X[n][i] = ldsr<v4u>(c.ax[i] + sl);
    else c.w1[n][(i - 4) >> 1][i & 1] = trrd(c.aw[0] + sl + 16384 + (((i - 4) >> 1) & 1) * 8192 + ((i - 4) >> 2) * 2048 + (i & 1) * 512);
  } else if constexpr (it.kind == P2 && T) {
    c.w2[n][i >> 1][i & 1] = trrd(c.aw2[(i >> 1) % 3] + sl + (i / 12) * 16384 + ((i / 6) & 1) * 4096 + (i & 1) * 512);
  } else if constexpr (it.kind == P2) {
    c.w2[n][i >> 1][i & 1] = trrd(c.aw[0] + sl + (i >> 1) * 2048 + (i & 1) * 512);
  } else if constexpr (it.kind == WW) {
    const v2u t = trrd(c.ab2[(i >> 1) & 1] + sl + (i >> 2) * 4096 + (i & 1) * 2048);
    c.b2[2 * n + (i >> 2)][(i >> 1) & 1][2 * (i & 1)] = t.x;
    c.b2[2 * n + (i >> 2)][(i >> 1) & 1][2 * (i & 1) + 1] = t.y;
  } else if constexpr (it.kind == AA) {
    if constexpr (n == 5) {
      const unsigned a = c.aa[i & 3] - c.lane * 8 + sl + (i >> 2) * 4096;
      c.af[n][i] = cat(ldsr<v2u>(a), ldsr<volatile v2u>(a + 512));
    } else if constexpr (i < 8) c.af[n][i] = ldsr<v4u>(c.aa[i & 3] + sl + (i >> 2) * 4096);
    else c.af[n + 1][i - 8] = ldsr<v4u>(((K & 1) ? c.aa[i & 3] ^ 65536u : c.aa[i & 3] + 32768u) + ((i >> 2) & 1) * 4096);
  }
}
template <int T, int K, int I, int E> DI void rd_range(St& c) {
  if constexpr (I < E) {
    rd<T, K, I>(c);
    rd_range<T, K, I + 1, E>(c);
  }
}
template <int T> constexpr int nrd(int k) {
  const int kd = item_at<T>(k).kind;
  return kd == P1 ? 20 : (kd == P2 ? (T ? 24 : 32) : (kd == WW || kd == AA ? (k == nitems<T>() - 2 ? 16 : 8) : 0));
}

struct Loc { v2u cf[4], nm[2]; unsigned ja; float mean, var; v4u g0, g1, f[16]; };

template <int T, int K, int part> DI void job(St& c, Loc& l) {
  constexpr int jb = item_at<T>(K).job;
  const unsigned lane = c.lane, g = c.g, r15 = c.r15, r31 = lane & 31, h = lane >> 5, row = 32 * c.m + r31;
  const f4 z4f = {0.f, 0.f, 0.f, 0.f};
  const unsigned q = r15 >> 2;
  if constexpr (!T && K + 1 == KA_RED && part == 3) {
    c.pre[0] = (KA_SL & 3) * 32768u + (8 * g + q) * 256 + (((2 * c.w + ((lane >> 1) & 1)) ^ ((q << 1) | ((g & 1) << 3))) << 4) + (lane & 1) * 8;
    c.pre[1] = L_GST + (8 * g + q) * 128 + ((((c.w & 6) + ((lane >> 1) & 1)) ^ (((q & 2) << 1) | ((g & 1) << 1) | (q & 1))) << 4) + (lane & 1) * 8;
    c.pre[2] = L_SDB + g * 16;
    asm volatile("" : "+v"(c.pre[0]), "+v"(c.pre[1]), "+v"(c.pre[2]));
  }
  if constexpr (!T && K == KA_RED) {
    if constexpr (part == -1) {
#pragma unroll
      for (int u = 0; u < 4; ++u) {
        l.f[u] = cat(trrd(c.pre[0] + u * 8192), trrd((c.pre[0] ^ 16) + u * 8192 + 1024));
        l.f[4 + u] = cat(trrd(c.pre[1] + u * 4096), trrd((c.pre[1] ^ 32) + u * 4096 + 512));
        l.f[8 + u] = ldsr<v4u>(c.pre[2] + u * 64);
      }
    }
    if constexpr (part == 1) {
      f4 kv = z4f, zz = z4f;
#pragma unroll
      for (int u = 0; u < 4; ++u) {
        kv = mfma32(l.f[u], l.f[4 + u], kv);
        zz = mfma32(l.f[8 + u], l.f[u], zz);
      }
      __builtin_amdgcn_raw_buffer_store_b64(pk4(kv), c.rws, lane * 8 + c.skv + c.ti * 512, 0, 16);
      if (lane < 16) __builtin_amdgcn_raw_buffer_store_b32(__float_as_uint(zz.x), c.rws, WS_Z + (c.jj * 8 + c.w) * 1024 + c.ti * 64 + lane * 4, 0, 16);
    }
  }
  if constexpr (!T && K >= KA_KL && K < KA_KL + KV_NW && part >= 0) {
#pragma unroll
    for (int k = ((K - KA_KL) * 4 + part) * 2 / KV_NW; k < ((K - KA_KL) * 4 + part + 1) * 2 / KV_NW; ++k)
      asm volatile("buffer_load_dwordx4 %0, %1, %2, %3 offen sc1" : "=v"(c.kp[k]) : "v"(c.vl), "s"(c.rws), "s"(c.skv + k * 1024) : "memory");
  }
  if constexpr (!T && item_at<T>(K + 1).job == JKV && part == 1) {
    const unsigned tb = (c.w >> 2) * 4 + (c.w & 1) * 2, e = (c.w >> 1) & 1, d = h | ((lane & 1) << 1) | ((lane & 2) << 2) | (lane & 4);
    const unsigned dg = r15 - 4 * g, one = (dg & 1) ? 0x3f800000u : 0x3f80u;
    c.pre[3] = L_GST + c.m1 + lane * 4;
    c.pre[4] = L_KVB + ((tb + (g & 1)) * 32 + 16 * e + d) * 16 + (lane & 8);
    c.pre[5] = L_ZB + ((tb + (r15 >> 3)) * 2 + e) * 16 + (lane & 7) * 2;
    c.pre[6] = (dg >> 1) == 0 ? one : 0u;
    c.pre[7] = (dg >> 1) == 1 ? one : 0u;
    asm volatile("" : "+v"(c.pre[3]), "+v"(c.pre[4]), "+v"(c.pre[5]), "+v"(c.pre[6]), "+v"(c.pre[7]));
  }
  if constexpr (jb == JKV) {
    if constexpr (part == -1) {
      constexpr int y = since<T>(KA_KL + KV_NW, K);
      asm volatile("s_waitcnt vmcnt(%8)"
                   : "+v"(c.kp[0]), "+v"(c.kp[1]), "+v"(c.kp[2]), "+v"(c.kp[3]), "+v"(c.kp[4]), "+v"(c.kp[5]), "+v"(c.kp[6]), "+v"(c.kp[7])
                   : "i"(y) : "memory");
#pragma unroll
      for (int k = 0; k < 4; ++k) l.g0[k] = ldsr<unsigned>(c.pre[3] + k * 256);
    }
    if constexpr (part == 0) {
      const v2u id = {c.pre[6], c.pre[7]};
      f4 s[2] = {z4f, z4f};
#pragma unroll
      for (int b = 0; b < 16; ++b) s[b & 1] = mfma16(id, v2u{c.kp[b >> 1][2 * (b & 1)], c.kp[b >> 1][2 * (b & 1) + 1]}, s[b & 1]);
#pragma unroll
      for (int r = 0; r < 4; ++r) {
        auto sw = __builtin_amdgcn_permlane32_swap(__float_as_uint(s[0][r]), __float_as_uint(s[1][r]), false, false);
        s[0][r] = __uint_as_float(sw[0]) + __uint_as_float(sw[1]);
      }
      const f4 zp = __builtin_bit_cast(f4, l.g0);
      const float z = xg_add((zp.x + zp.y) + (zp.z + zp.w));
      ldsw<uint16_t>(c.pre[5], (uint16_t)pk2(z, 0.f));
      ldsw<v2u>(c.pre[4], pk4(s[0]));
    }
    if constexpr (part == 1) {
      const bool on = (r31 >> 4) == h;
      c.pre[0] = on ? L_KVB + (c.hf * 128 + r31) * 16 : L_ZERO;
      c.pre[1] = on ? L_ZB + c.hf * 128 + h * 16 : L_ZERO;
      c.pre[2] = WS_A2 + ((2 * c.ti + (c.m >> 1)) * 72 + 9 * c.jj + 7 + c.hf) * 4096 + (2 * (c.m & 1) + (r31 >> 4)) * 1024 + (r15 + 16 * h) * 16;
      c.sd = ldsr<float>(L_STD + row * 4);
      asm volatile("" : "+v"(c.pre[0]), "+v"(c.pre[1]), "+v"(c.pre[2]));
    }
  }
  if constexpr (jb == JO) {
    if constexpr (part == -1) {
#pragma unroll
      for (int t = 0; t < 4; ++t) {
        l.f[t] = ldsr<v4u>(c.pre[1] + t * 32);
        l.f[4 + t] = ldsr<v4u>(c.pre[0] + t * 512);
      }
    }
    if constexpr (part == 1) {
      f16v num = zero16(), den = zero16();
#pragma unroll
      for (int t = 0; t < 4; ++t) den = mfmaB(l.f[t], c.ph[t], den);
#pragma unroll
      for (int t = 0; t < 4; ++t) num = mfmaB(l.f[4 + t], c.ph[t], num);
      const float r0 = __builtin_amdgcn_rcpf(den[0] + 1e-6f) * c.sd, r1 = __builtin_amdgcn_rcpf(den[8] + 1e-6f) * c.sd;
      const f8v lo = __builtin_shufflevector(num, num, 0, 1, 2, 3, 4, 5, 6, 7) * r0, hi = __builtin_shufflevector(num, num, 8, 9, 10, 11, 12, 13, 14, 15) * r1;
      __builtin_amdgcn_raw_buffer_store_b128(v4u{pk2(lo[0], lo[1]), pk2(lo[2], lo[3]), pk2(lo[4], lo[5]), pk2(lo[6], lo[7])}, c.rws, c.pre[2], 0, 16);
      __builtin_amdgcn_raw_buffer_store_b128(v4u{pk2(hi[0], hi[1]), pk2(hi[2], hi[3]), pk2(hi[4], hi[5]), pk2(hi[6], hi[7])}, c.rws, c.pre[2] + 512, 0, 16);
    }
  }
  if constexpr (jb == JG) {
    const unsigned w = c.w, tg = (unsigned)c.tag;
    auto fetch = [&]() {
      const unsigned a = L_GST + c.m1 + lane * 8;
      l.g0 = cat(ldsr<v2u>(a), ldsr<v2u>(a + 512));
      l.nm[0] = ldsr<v2u>(l.ja + (L_SST - L_MISC) + (w & 1) * 1536);
    };
    if constexpr (part == 0) {
      l.ja = misc(lane * 8);
      fetch();
    }
    if constexpr (part == 2) {
#pragma unroll 1
      for (unsigned spins = 0; spins < SPIN_CAP; ++spins) {
        const unsigned bad = ((l.g0.y ^ tg) | (l.g0.w ^ tg)) & (T && w == 7 ? 0u : ~0u);
        if (__all((bad | ((l.nm[0].y ^ tg) & (w < 2 ? ~0u : 0u))) == 0)) break;
        gload<T>(c);
        WAIT_VM(0);
        fetch();
      }
      const unsigned sh = __float_as_uint(__uint_as_float(l.g0.x) + __uint_as_float(l.g0.z));
      auto sw = __builtin_amdgcn_permlane32_swap(sh, sh, false, false);
      const float s = __uint_as_float(sw[0]) + __uint_as_float(sw[1]);
      const float hi = rbf(s), mid = rbf(s - hi);
      ldsw<unsigned>(L_CSB + w * 256 + r31 * 8 + h * 4, pk2(h ? (s - hi) - mid : hi, h ? 0.f : mid));
      if (w < 2) {
        const unsigned gx = l.nm[0].x, nm = (gx & 0xffffu) ^ 0x8000u, tid = w * 64 + lane;
        ldsw<v2u>(l.ja + (L_STM - L_MISC) + w * 512, v2u{nm | (nm << 16), nm});
        if (!T) {
          ldsw<float>(L_STR + tid * 4, __builtin_amdgcn_rcpf(bhi(gx)));
          ldsw<float>(L_STD + tid * 4, bhi(gx));
          ldsw<uint16_t>(L_SDB + tid * 2, (uint16_t)(gx >> 16));
        }
      }
    }
  }
  if constexpr (K == CS_W) {
    if constexpr (part == 3) {
      const unsigned w = c.w, u = (c.ti >> 2) * 4 + w - (T ? (c.jj & 1) * 2 : 0);
      f4 s = z4f;
      if (w < 4) {
        const unsigned a = L_GST + (8 * g + q) * 128 + (lane & 3) * 8 + ((w ^ ((q & 2) | (g & 1))) << 5);
        const v4u ones = {0x3f803f80u, 0x3f803f80u, 0x3f803f80u, 0x3f803f80u};
#pragma unroll
        for (int k = 0; k < 4; ++k) s = mfma32(ones, trfrag(a + k * 4096, 512), s);
      }
      const unsigned col = T ? 1536 + 224 * c.jj + 16 * u : (u >> 2) * 512 + 64 * c.jj + w * 16;
      if (lane < 16) __builtin_amdgcn_raw_buffer_store_b64(v2u{__float_as_uint(s.x), (unsigned)c.tag}, c.rws,
                                                           (w < 4 && u < (T ? 14u : 12u) ? WS_CS + col * 8 + (c.ti & 3) * 32768 : WS_END) + lane * 8, 0, 16);
    }
  }
  if constexpr (jb == JK1 || jb == JQ1) {
    constexpr bool isk = jb == JK1;
    if constexpr (part == 0) {
      const unsigned a8 = misc(r31 * 8);
      l.nm[0] = ldsr<v2u>(a8 + c.m * 256 + (L_STM - L_MISC));
      l.ja = a8 + c.hf * 256 + (L_CSB - L_MISC);
      c.sc = ldsr<float>(L_STR + row * 4) * 1.4426950408889634f;
      if (h) l.nm[0] = v2u{0, 0};
      if constexpr (isk) {
        mm_a1<7, 0>(c); mm_a1<7, 2>(c); mm_a1<7, 4>(c); mm_a1<7, 6>(c);
        c.kk = mfmaB8(ldsr<v2u>(l.ja + 512), l.nm[0], c.kk);
      } else {
        mm_range<T, mm_done<T>(K), mm_done<T>(K + 1)>(c);
        c.kk = mfmaB8(ldsr<v2u>(l.ja), l.nm[0], c.qq);
      }
    }
    if constexpr (part == 1) {
      const unsigned wa = (lane & 1) * 512 + (4 * h + (r15 >> 2)) * 32 + (2 * (g & 1) + ((lane >> 1) & 1)) * 8;
      const v4u wu = trfrag(misc(c.hf * 1024 + wa) + (L_WPHI - L_MISC), 256);
      if constexpr (isk) {
        mm_a1<7, 1>(c); mm_a1<7, 3>(c); mm_a1<7, 5>(c); mm_a1<7, 7>(c);
        c.vv = mfmaB8(ldsr<v2u>(l.ja + 1024), l.nm[0], c.vv);
      }
      const v4u z4 = {0, 0, 0, 0};
      c.kk = mfmaB((lane & 4) ? wu : z4, pk8(c.kk, 8), mfmaB((lane & 4) ? z4 : wu, pk8(c.kk, 0), zero16()));
    }
    if constexpr (part == 2) {
      if constexpr (isk) {
        const unsigned a = L_GST + row * 128 + (((4 * c.hf + h) ^ (((r31 & 2) << 1) | (((r31 >> 2) ^ (r31 >> 1)) & 2) | (r31 & 1))) << 4);
        ldsw<v4u>(a, pk8(c.vv, 0));
        ldsw<v4u>(a ^ 32, pk8(c.vv, 8));
      }
      c.sa = -max16(c.kk) * c.sc;
    }
    if constexpr (part == 3) {
      c.sb = min16(c.kk) * c.sc;
      asm volatile("" : "+v"(c.sa), "+v"(c.sb), "+v"(c.sc));
    }
  }
  if constexpr (jb == JQ2 && part >= 0) {
    softmax_exp(c.kk, c.sc, c.sa, c.sb, c.se, c.sn, 4 * part);
    if constexpr (part == 3) asm volatile("" : "+v"(c.se), "+v"(c.sn));
  }
  if constexpr (jb == JQ3) {
    if constexpr (part == 0) softmax_norm(c.se, 1.f, c.ph[0], c.ph[1]);
    if constexpr (part == 2) softmax_norm(c.sn, 1.f, c.ph[2], c.ph[3]);
    if constexpr (part == 3) asm volatile("" : "+v"(c.ph[0]), "+v"(c.ph[1]), "+v"(c.ph[2]), "+v"(c.ph[3]));
  }
  if constexpr (jb == JK3 && (part == 0 || part == 1)) {
    softmax_exp(c.kk, c.sc, c.sa, c.sb, c.se, c.sn, 8 * part);
    softmax_exp(c.kk, c.sc, c.sa, c.sb, c.se, c.sn, 8 * part + 4);
  }
  if constexpr (jb == JK3 && part >= 2) {
    v4u p0, p1;
    softmax_norm(part == 3 ? c.sn : c.se, c.sc * 0.6931471805599453f, p0, p1);
    const unsigned a = (KA_SL & 3) * 32768u + row * 256 + (((8 * c.hf + 4 * h + 2 * (part & 1)) ^ (((r31 & 3) << 1) | (r31 & 8) | ((r31 >> 2) & 1))) << 4);
    ldsw<v4u>(a, p0);
    ldsw<v4u>(a ^ 16, p1);
  }
}

template <int T, int K, int i> DI void slots(St& c, Loc& l) {
  constexpr bool own = item_at<T>(K).job == JQ1;
  constexpr int Q0 = mm_done<T>(K), NM = own ? 0 : mm_done<T>(K + 1) - Q0, NR = nrd<T>(K);
  constexpr bool QI = !T && item_at<T>(K).kind == P2 && NM == 8;
  static_assert(!QI || mm_op<T>(Q0).k == K, "");
  static_assert(T || item_at<T>(K).kind != P2 || QI || (NM == 16 && mm_op<T>(Q0 + 8).k == K && mm_op<T>(Q0 + 8).j == 0), "");
  constexpr int NS = QI ? 12 : (NM < 4 ? 4 : NM);
  constexpr int RS = NS > 4 ? NS - 2 : NS;
  constexpr bool EP = T && (K == KB_E1 || K == KB_E2);
  if constexpr (i < NS) {
    if constexpr (QI) {
      if constexpr (i >= 4) mm<T, Q0 + i - 4>(c);
      rd_range<T, K, (i < 4 ? 4 * i : 8 + 2 * i), (i < 4 ? 4 * i + 4 : 10 + 2 * i)>(c);
    } else if constexpr (i < NM) {
      mm<T, Q0 + i>(c);
    }
    if constexpr (QI) {
    } else if constexpr (item_at<T>(K).job == JK1 || item_at<T>(K).job == JK3 || item_at<T>(K).job == JO) { if constexpr (i == 3) rd_range<T, K, 0, NR>(c); }
    else if constexpr (i < RS && !(K == 0 && i > 0)) rd_range<T, K, (NR * i) / RS, (NR * (i + 1)) / RS>(c);
    if constexpr (EP && i == NS - 3) {
      constexpr bool e1 = K == KB_E1;
      const unsigned ca = misc((((e1 ? (c.jj & 1) * 6 : 8 - (c.jj & 1) * 8) + 4 * c.hf) * 16 + c.r15) * 8), na = misc((32 * c.m + c.r15) * 8);
#pragma unroll
      for (int t = 0; t < (e1 ? 4 : 3); ++t) l.cf[t] = ldsr<v2u>(ca + (L_CSB - L_MISC) + t * 128 - (!e1 && t == 2 ? c.hf * 384 : 0));
#pragma unroll
      for (int mi = 0; mi < 2; ++mi) l.nm[mi] = ldsr<v2u>(na + (L_STM - L_MISC) + mi * 128);
    }
    if constexpr (i % (NS / 4) == (NS >= 8 ? 1 : 0) && i / (NS / 4) < 4) {
      if constexpr (K == 0 && i == 0) rd_range<T, K, NR / RS, NR>(c);
      if constexpr (!T && K == 2 && i < NS / 4) { if (c.w < 2) dma<L_WPHI, 0>(c.m1, c.vl, c.rphi, c.jj * 2048 + c.m1); }
      request<T, K>(c, i / (NS / 4));
      job<T, K, i / (NS / 4)>(c, l);
    }
    SCHED();
    slots<T, K, i + 1>(c, l);
  }
}

template <int T, int K> DI void window(St& c) {
  constexpr Item it = item_at<T>(K);
  constexpr unsigned hi = (K & 2) * 32768u;
  const unsigned lane = c.lane, w = c.w, g = c.g, r15 = c.r15, r31 = lane & 31, h = lane >> 5, q = r15 >> 2;
  const f4 z4f = {0.f, 0.f, 0.f, 0.f};
  WAIT_LGKM0();
  constexpr int alw = allowed<T>(K);
  WAIT_VM(alw);
  if constexpr (K == ev<T>(0).fw) { if (lane == 0) set_flag(c.ws, T ? WS_FA + (c.ti * 24 + c.jj) * 64 + w * 8 : WS_FKV + (c.jj * 8 + w) * 128 + c.ti * 8, c.tag); }
  if constexpr (K == ev<T>(1).fw) { if (lane == 0) set_flag(c.ws, WS_FA + (c.ti * 24 + (T ? 8 : 16) + c.jj) * 64 + w * 8, c.tag); }
  BARRIER();
  SCHED();
  Loc l;
  job<T, K, -1>(c, l);
  SCHED();
  constexpr int g0 = gate_in<T>(K), g1 = gate_in<T>(K + 1), g2 = gate_in<T>(K + 2);
  if constexpr (g0 >= 0) {
    gate<nreq<T>(K - leads[4 * T + (g0 & 3)]) + since<T>(K - leads[4 * T + (g0 & 3)] + 1, K) + (K - leads[4 * T + (g0 & 3)] == (T ? XR_B : XR_A) ? 2 : 0)>(c, c.gate[g0], gate_addr(c, g0));
    if constexpr (g0 == 3) {
      dma<L_GST, WS_Z, true>(c.m1, c.vl, c.rws, (c.jj * 8 + w) * 1024);
    }
  }
  if constexpr (g1 >= 0 && leads[4 * T + (g1 & 3)] == 1) snapshot(c, c.gate[g1], gate_addr(c, g1));
  if constexpr (g2 >= 0 && leads[4 * T + (g2 & 3)] == 2) snapshot(c, c.gate[g2], gate_addr(c, g2));
  if constexpr (T && K == PF_LATE) {
    touch(c.rw2, (((c.ti * 8 + c.jj) * 8 + w) * 14 + (lane < 14 ? lane : 0)) * 128);
  }
  if constexpr (item_at<T>(K + G_LEAD).job == JG) gload<T>(c);
#define FOLLOW(arr, cnt, prev)                                                            \
  if constexpr ((prev) >= 0 && (((prev) ^ K) & 2)) {                                      \
    _Pragma("unroll") for (int t = 0; t < cnt; ++t) { arr[t] ^= 65536u; PIN(arr[t]); }    \
  }
  if constexpr (it.kind == P1) { FOLLOW(c.ax, (T ? 2 : 4), last_use<T>(K, P1, P1)) }
  if constexpr (it.kind == P1 || (!T && it.kind == P2)) { FOLLOW(c.aw, (T ? 4 : 1), last_use<T>(K, P1, T ? P1 : P2)) }
  if constexpr (T && it.kind == P2) {
    FOLLOW(c.aw2, 3, last_use<T>(K, P2, P2))
    if constexpr (it.n == 0) {
#pragma unroll
      for (int t = 0; t < 3; ++t) {
        const unsigned lu = (c.jj & 1) * 2 + (t < 2 ? 4 * c.hf + t : 2 + c.hf);
        c.aw2[t] = hi + (lu >> 2) * 8192 + (8 * g + q) * 128 + (lane & 3) * 8 + (((lu & 3) ^ ((q & 2) | (g & 1))) << 5);
        PIN(c.aw2[t]);
      }
    }
  }
  if constexpr (it.kind == WW) {
    FOLLOW(c.ab2, 2, last_use<T>(K, WW, WW))
    if constexpr (it.n == 0) {
#pragma unroll
      for (int nf = 0; nf < 2; ++nf) {
        c.ab2[nf] = hi + (w >> 1) * 8192 + (4 * g + q) * 128 + (((2 * (w & 1) + nf) ^ ((q >> 1) | ((g & 1) << 1))) << 5) + (lane & 3) * 8;
        PIN(c.ab2[nf]);
      }
    }
  }
  if constexpr (it.kind == AA) {
    FOLLOW(c.aa, 4, last_use<T>(K, AA, AA))
    if constexpr (it.n == 0) {
#pragma unroll
      for (int s = 0; s < 4; ++s) { c.aa[s] = hi + (w >> 1) * 8192 + ((s ^ (w >> 1)) << 10) + lane * 16; PIN(c.aa[s]); }
    }
  }
  slots<T, K, 0>(c, l);
  if constexpr (K == (T ? XR_B : XR_A)) {
    dma<L_XR, 0>(c.m1, c.vb2, c.rx, (c.r2 * 64 + 8 * w) * 1024);
    unsigned t;
    asm volatile("s_add_u32 m0, %1, %2\n\ts_add_u32 %0, %3, %4\n\tbuffer_load_dword %5, %6, %0 offen sc1 lds"
                 : "=&s"(t) : "s"(w * 256), "i"(L_SST), "s"((c.r2 * 64 + (w >> 1) * 16) * 8), "i"(WS_ST), "v"((lane & 31) * 4), "s"(c.rws) : "memory", "scc");
  }
  if constexpr (T && (K == KB_E1 || K == KB_E2)) {
    constexpr bool e1 = K == KB_E1;
    const unsigned ob = lane * 16 + WS_A2 + ((2 * c.ti + (c.m >> 1)) * 72 + 9 * c.jj) * 4096 + (c.m & 1) * 2048;
#pragma unroll
    for (int mi = 0; mi < 2; ++mi) {
      if (g) l.nm[mi] = v2u{0, 0};
      v2u hp[4];
#pragma unroll
      for (int t = 0; t < (e1 ? 4 : 3); ++t) {
        const v2u pk = pk4(mfma16(l.cf[t], l.nm[mi], e1 ? c.acc[t][mi] : c.acc1[t % 3][mi]));
        hp[t] = v2u{relu2(pk.x), relu2(pk.y)};
      }
      if constexpr (e1) {
        const unsigned o = ob + 2 * c.hf * 4096 + mi * 1024;
        __builtin_amdgcn_raw_buffer_store_b128(cat(hp[0], hp[1]), c.rws, o, 0, 16);
        __builtin_amdgcn_raw_buffer_store_b128(cat(hp[2], hp[3]), c.rws, o + 4096, 0, 16);
      } else {
        const unsigned o = ob + 4 * 4096 + mi * 1024;
        __builtin_amdgcn_raw_buffer_store_b128(cat(hp[0], hp[1]), c.rws, o + c.hf * 8192, 0, 16);
        __builtin_amdgcn_raw_buffer_store_b64(hp[2], c.rws, o + 4096 + c.hf * 512 - lane * 8, 0, 16);
      }
    }
    SCHED();
  }
}

template <int T, int K> DI void windows(St& c) {
  window<T, K>(c);
  if constexpr (K + 2 < nitems<T>()) windows<T, K + 1>(c);
}

template <int T> DI void program(St& c, uint16_t* out) {
  const unsigned lane = c.lane, w = c.w, g = c.g, r15 = c.r15;
  const unsigned q = r15 >> 2;
  if (T) {
    const unsigned f = (q & 2) | (g & 1), rowp = (8 * g + q) * 128 + (lane & 3) * 8;
#pragma unroll
    for (int ks = 0; ks < 2; ++ks) c.ax[ks] = (4 * c.m + (r15 >> 3)) * 1024 + (r15 & 7) * 128 + (((4 * ks + g) ^ (r15 >> 1)) << 4);
#pragma unroll
    for (int t = 0; t < 4; ++t) c.aw[t] = 16384 + c.hf * 8192 + rowp + ((t ^ f) << 5);
    asm volatile("" : "+v"(c.ax[0]), "+v"(c.ax[1]), "+v"(c.aw[0]), "+v"(c.aw[1]), "+v"(c.aw[2]), "+v"(c.aw[3]));
  } else {
    const unsigned r31 = lane & 31, h = lane >> 5;
#pragma unroll
    for (int u = 0; u < 4; ++u) c.ax[u] = (4 * c.m + (r31 >> 3)) * 1024 + (r31 & 7) * 128 + (((2 * u + h) ^ ((r31 >> 1) & 7)) << 4);
    c.aw[0] = (8 * h + q) * 128 + (lane & 3) * 8 + (((2 * c.hf + (g & 1)) ^ ((q & 2) | h)) << 5);
    asm volatile("" : "+v"(c.ax[0]), "+v"(c.ax[1]), "+v"(c.ax[2]), "+v"(c.ax[3]), "+v"(c.aw[0]));
    ldsw<unsigned>(L_ZERO + (w * 64 + lane) * 4, 0u);
  }

  windows<T, 0>(c);

  const unsigned nh = w & 1, kq = w >> 1;
  constexpr int N = nitems<T>();
  static_assert(item_at<T>(N - 2).kind == AA && item_at<T>(N - 2).n == 7 && mm_op<T>(mm_done<T>(N - 1)).k == N - 2 && mm_op<T>(mm_done<T>(N - 1)).j == 0, "");
  constexpr unsigned rb0 = (N & 3) * 32768u, rb1 = ((N + 1) & 3) * 32768u;
  auto fin = [&](int s) {
#pragma unroll
    for (int j = 0; j < 8; ++j) c.acc2[s][j & 1] = mfma32(c.b2[14 + (j >> 1)][j & 1], c.af[7 + (j >> 2)][4 * ((j >> 1) & 1) + s], c.acc2[s][j & 1]);
  };
  auto put = [&](int s) {
    const unsigned d = (kq ^ s) * 2 + nh, a = (d & 4 ? rb1 : rb0) + ((d & 3) * 3 + (s - 1)) * 2048 + c.vl;
    ldsw<f4>(a, c.acc2[s][0]);
    ldsw<f4>(a + 1024, c.acc2[s][1]);
  };
  c.gst = ldsr<v2u>(L_SST + w * 256 + r15 * 8);
#pragma unroll
  for (int nf = 0; nf < 2; ++nf) c.xr[nf] = ldsr<v2u>(L_XR + (kq * 16 + r15) * 128 + (((2 * nh + nf) ^ ((r15 >> 1) & 3)) << 5) + g * 8);
  fin(1);
  if (__any(c.gst.y != (unsigned)c.tag)) c.gst = __builtin_amdgcn_raw_buffer_load_b64(c.rws, WS_ST + (c.r2 * 64 + kq * 16 + r15) * 8, 0, 16);
  unsigned ra = (w & 4 ? rb1 : rb0) + (w & 3) * 6144 + c.vl;
  unsigned oa = (c.r2 * 64 + kq * 16 + r15) * 1024 + (c.c2 * 64 + nh * 32 + (g & 1) * 16 + (g >> 1) * 8) * 2;
  float rs = __builtin_amdgcn_rcpf(bhi(c.gst.x));
  f4 xf[2];
#pragma unroll
  for (int nf = 0; nf < 2; ++nf) xf[nf] = f4{blo(c.xr[nf].x), bhi(c.xr[nf].x), blo(c.xr[nf].y), bhi(c.xr[nf].y)};
  asm volatile("" : "+v"(ra), "+v"(oa), "+v"(rs), "+v"(xf[0]), "+v"(xf[1]));
  SCHED();
  fin(2);
  __builtin_amdgcn_sched_group_barrier(0x8, 2, 0);
  put(1);
  SCHED();
  fin(3);
  __builtin_amdgcn_sched_group_barrier(0x8, 2, 0);
  put(2);
  SCHED();
  fin(0);
  __builtin_amdgcn_sched_group_barrier(0x8, 2, 0);
  put(3);
  SCHED();
  WAIT_LGKM0();
  BARRIER();
  f4 pr[6];
#pragma unroll
  for (int k = 0; k < 6; ++k) pr[k] = ldsr<f4>(ra + k * 1024);
  SCHED();
  v2u o[2];
#pragma unroll
  for (int nf = 0; nf < 2; ++nf) o[nf] = pk4(__builtin_elementwise_fma((c.acc2[0][nf] + pr[nf]) + (pr[2 + nf] + pr[4 + nf]), f4{rs, rs, rs, rs}, xf[nf]));
  auto sx = __builtin_amdgcn_permlane16_swap(o[0].x, o[1].x, false, false);
  auto sy = __builtin_amdgcn_permlane16_swap(o[0].y, o[1].y, false, false);
  __builtin_amdgcn_raw_buffer_store_b128(v4u{sx[0], sy[0], sx[1], sy[1]}, c.rout, oa, 0, 16);
  if (blockIdx.x == 0 && w == 0 && lane == 0) set_flag(c.ws, WS_EPOCH, c.tag);
}

typedef u64 v8q __attribute__((ext_vector_type(8)));
__global__ __launch_bounds__(512, 2) void fused_block(const uint16_t*, const uint16_t*, const uint16_t*, const uint16_t*, const uint16_t*,
                                                      const uint16_t*, uint16_t*, char*) {
  __shared__ __attribute__((aligned(1024))) char smem[LDS_BYTES];
  v8q ka;
  asm volatile("s_load_dwordx16 %0, %1, 0x0" : "=s"(ka) : "s"(__builtin_amdgcn_kernarg_segment_ptr()), "s"((unsigned)(uintptr_t)(LAS char*)smem));
  St c;
  const unsigned b = blockIdx.x, xcd = b & 7, idx = b >> 3;
  {
    u64 pc;
    asm volatile("s_getpc_b64 %0" : "=s"(pc));
    touch(mk_rsrc((const void*)(pc & ~127ull), CODE_PF), (idx * 8 + (threadIdx.x >> 6)) * 128);
  }
  const unsigned lane = threadIdx.x & 63, w = rfl(threadIdx.x >> 6), type = (idx >> 1) & 1;
  c.lane = lane;
  c.w = w;
  c.m = w & 3;
  c.hf = w >> 2;
  c.g = lane >> 4;
  c.r15 = lane & 15;
  c.ti = (xcd & 1) * 8 + (idx >> 2);
  c.jj = (xcd >> 1) * 2 + (idx & 1);
  c.r2 = xcd * 4 + (idx >> 3);
  c.c2 = idx & 7;
  c.m1 = w * 1024;
  c.m2 = w * 2048;
  c.m4 = w * 4096;
  c.sw1 = (224 * w + 96 * (w & 1)) * 1024;
  c.sw2 = (224 * w + 128 - 128 * (w & 1)) * 1024;
  c.swo = w * 65536;
  c.sa2 = WS_A2 + (c.r2 * 72 + 9 * w) * 4096;
  c.skv = WS_KV + (c.jj * 8 + w) * 8192;
  c.vl = lane * 16;
  c.vb2 = (lane >> 3) * 1024u + c.c2 * 128u + (((lane & 7) ^ (((lane >> 4) & 3) << 1)) << 4);
  const unsigned wstr = 3072u + 512u * type, img = type ? 128u : 1024u;
  c.vx0 = (c.ti * 128 + 16 * w + (lane >> 3)) * 1024u + (((lane & 7) ^ (lane >> 4)) << 4);
  c.vx1 = (c.vx0 ^ 64u) + 8192u;
  c.vw = (8 * w + (lane >> 3)) * wstr + (((128u + 320u * type) * c.jj) & ~127u) + (((lane & 7) ^ ((((lane >> 3) & 2) | (w & 1)) << 1)) << 4);
  c.sp1 = type ? (c.jj & 1) * 256 : 1024u;
  c.sp2 = type ? 256 - (c.jj & 1) * 256 : 0u;
  unsigned srow = (c.ti * 128 + (type * 8 + c.jj) * 8 + w) * 1024, s01 = c.sp1 + img, s10 = c.sp1 + 64 * wstr, s11 = s10 + img;
  unsigned sc0 = !type && c.ti >= 12 ? 0x40000000u : (c.ti & 3) * 128 * wstr + (c.ti >> 2) * img, sc1 = sc0 + 64 * wstr;
  const unsigned id = c.ti * 8 + c.jj;
  unsigned po = lane < 4 ? WS_Z + (id * 4 + lane) * 128 : WS_ST + (id * 3 + (lane & 3)) * 128;
  if (w == 2) po = (id * 32 + (lane & 31)) * 128;
  asm volatile("" : "+v"(c.vl), "+v"(c.vx0), "+v"(c.vx1), "+v"(c.vw), "+v"(po));
  asm volatile("s_waitcnt lgkmcnt(0)"
               : "+s"(ka), "+s"(c.m1), "+s"(c.m2), "+s"(c.sp1), "+s"(srow), "+s"(s01), "+s"(s10), "+s"(s11), "+s"(sc0), "+s"(sc1));
  char* ws = (char*)ka[7];
  uint16_t* out = (uint16_t*)ka[6];
  c.rx = mk_rsrc((const void*)ka[0], 2048u * 1024u);
  c.rw = mk_rsrc((const void*)(type ? ka[4] : ka[1]), 512u * wstr);
  asm volatile("s_load_dwordx2 %0, %1, 0x0" : "=s"(c.tag) : "s"(ws) : "memory");
  asm volatile("buffer_load_dwordx4 %0, %1, %2, %3 offen" : "=v"(c.xrow) : "v"(c.vl), "s"(c.rx), "s"(srow) : "memory");
  static_assert(seqA[0].kind == P1 && seqA[1].kind == P1 && seqB[0].kind == P1 && seqB[1].kind == P1 && seqA[1].n == 1 && seqB[1].n == 1, "");
  dma<0, 0>(c.m2, c.vx0, c.rx, 0);
  dma<1024, 0>(c.m2, c.vx1, c.rx, 0);
  dma<16384, 0>(c.m1, c.vw, c.rw, c.sp1);
  dma<24576, 0>(c.m1, c.vw, c.rw, s01);
  dma<32768, 128>(c.m2, c.vx0, c.rx, 0);
  dma<32768 + 1024, 128>(c.m2, c.vx1, c.rx, 0);
  dma<32768 + 16384, 0>(c.m1, c.vw, c.rw, s10);
  dma<32768 + 24576, 0>(c.m1, c.vw, c.rw, s11);
  c.rws = mk_rsrc(ws, WS_END);
  {
    asm volatile("s_waitcnt vmcnt(8)" : "+v"(c.xrow)::"memory");
    float v[8];
#pragma unroll
    for (int d = 0; d < 4; ++d) { v[2 * d] = blo(c.xrow[d]); v[2 * d + 1] = bhi(c.xrow[d]); }
    const float mean = wave_add(((v[0] + v[1]) + (v[2] + v[3])) + ((v[4] + v[5]) + (v[6] + v[7]))) * (1.f / 512.f);
    float q0 = 0.f, q1 = 0.f;
#pragma unroll
    for (int d = 0; d < 4; ++d) {
      q0 = __builtin_fmaf(v[2 * d] - mean, v[2 * d] - mean, q0);
      q1 = __builtin_fmaf(v[2 * d + 1] - mean, v[2 * d + 1] - mean, q1);
    }
    float sd = __builtin_amdgcn_sqrtf(rbf(rbf(wave_add(q0 + q1) * (1.f / 512.f)) + 1e-5f));
    PIN(sd);
    dma<L_GST, 0>(c.m1, c.vw, c.rw, sc0);
    dma<L_GST + 8192, 0>(c.m1, c.vw, c.rw, sc1);
    asm volatile("s_waitcnt lgkmcnt(0)" : "+s"(c.tag));
    c.tag += 1;
    if (lane == 0) __builtin_amdgcn_raw_buffer_store_b64(v2u{pk2(mean, sd), (unsigned)c.tag}, c.rws, WS_ST + (srow >> 7), 0, 16);
  }
  c.rw2 = mk_rsrc((const void*)ka[5], 1792u * 1024u);
  c.rwo = mk_rsrc((const void*)ka[3], 512u * 1024u);
  c.rphi = mk_rsrc((const void*)ka[2], 32u * 512u);
  c.rws = mk_rsrc(ws, WS_END);
  c.ws = ws;
  c.rout = mk_rsrc(out, 2048u * 1024u);
  if (type) {
    touch(w == 2 ? c.rwo : mk_rsrc(ws, w == 1 ? WS_END : 0u), po);
    program<1>(c, out);
  } else {
    program<0>(c, out);
  }
}

extern "C" void kernel_launch(void* const* d_in, const int* in_sizes, int n_in, void* d_out, int out_size, void* d_ws,
                              size_t ws_size, hipStream_t stream) {
  fused_block<<<256, 512, 0, stream>>>((const uint16_t*)d_in[0], (const uint16_t*)d_in[1], (const uint16_t*)d_in[2],
                                       (const uint16_t*)d_in[3], (const uint16_t*)d_in[5], (const uint16_t*)d_in[6],
                                       (uint16_t*)d_out, (char*)d_ws);
}
```
